```python
import torch

def _softmax(x, axis=-1):
    return torch.softmax(x, dim=axis)
def _gelu(x):
    return torch.nn.functional.gelu(x, approximate="tanh")
def _silu(x):
    return torch.nn.functional.silu(x)
def _sigmoid(x):
    return torch.sigmoid(x)
def _layernorm(x, axis=-1, eps=1e-5):
    m = x.mean(dim=axis, keepdim=True)
    v = x.var(dim=axis, keepdim=True, unbiased=False)
    return (x - m) / torch.sqrt(v + eps)
def _scatter_add(tgt, idx, src, axis=0):
    return tgt.clone().index_add_(axis, idx, src)
def _segment_sum(src, seg, n):
    out = torch.zeros((n,) + src.shape[1:], dtype=src.dtype)
    return out.index_add_(0, seg, src)
def _pad(x, pw):
    flat = tuple(v for pair in reversed(pw) for v in pair)
    return torch.nn.functional.pad(x, flat)

kernel_name = 'parallel_sum__sliding_block__glu_gated_bilinear__prompt'

def setup_inputs():

    _w = (lambda a: (lambda t: t.to(torch.bfloat16) if t.is_floating_point() else t)(torch.as_tensor(a)))
    def _A_setup():
        B, S, D = 1, 2048, 1024
        return {
            "x": _w((lambda *s: torch.randn(*s, dtype=torch.bfloat16) * 0.05)(B, S, D)),
            "wqkv": _w((lambda *s: torch.randn(*s, dtype=torch.bfloat16) * 0.05)(D, 3 * D)),
            "wo": _w((lambda *s: torch.randn(*s, dtype=torch.bfloat16) * 0.05)(D, D)),
        }
    _A_in = _A_setup()

    def _B_setup():
        B, S, D, F = 1, 2048, 1024, 3584
        return {
            "x": _w((lambda *s: torch.randn(*s, dtype=torch.bfloat16) * 0.05)(B, S, D)),
            "w_gate": _w((lambda *s: torch.randn(*s, dtype=torch.bfloat16) * 0.8)(D, F) * (D ** -0.5)),
            "w_up": _w((lambda *s: torch.randn(*s, dtype=torch.bfloat16) * 0.8)(D, F) * (D ** -0.5)),
            "w_down": _w((lambda *s: torch.randn(*s, dtype=torch.bfloat16) * 0.8)(F, D) * (F ** -0.5)),
        }
    _B_in = _B_setup()

    return {
        "A_x": _A_in["x"],
        "A_wqkv": _A_in["wqkv"],
        "A_wo": _A_in["wo"],
        "B_x": _B_in["x"],
        "B_w_gate": _B_in["w_gate"],
        "B_w_up": _B_in["w_up"],
        "B_w_down": _B_in["w_down"],
    }

def reference(A_x, A_wqkv, A_wo, B_x, B_w_gate, B_w_up, B_w_down):
    def _A_ref(x, wqkv, wo):
        B, S, D = x.shape
        H = 32; Dh = D // H; W = 256
        qkv = (lambda x, *d: x.permute(*d))((x @ wqkv).reshape(B, S, 3, H, Dh), 2, 0, 3, 1, 4)
        q, k, v = qkv[0], qkv[1], qkv[2]
        scores = torch.einsum("bhqd,bhkd->bhqk", q, k) * (Dh ** -0.5)
        idx = torch.arange(S)
        mask = (idx[:, None] - idx[None, :] >= 0) & (idx[:, None] - idx[None, :] < W)
        scores = torch.where(mask[None, None], scores, -1e9)
        p = _softmax(scores, axis=-1)
        o = torch.einsum("bhqk,bhkd->bhqd", p, v)
        return (lambda x, *d: x.permute(*d))(o, 0, 2, 1, 3).reshape(B, S, D) @ wo

    def _B_ref(x, w_gate, w_up, w_down):
        g = torch.einsum("bsd,df->bsf", x, w_gate)
        u = torch.einsum("bsd,df->bsf", x, w_up)
        h = g * u
        return torch.einsum("bsf,fd->bsd", h, w_down)

    x = A_x
    xn = _layernorm(x, axis=-1)
    return x + _A_ref(xn, A_wqkv, A_wo) + _B_ref(xn, B_w_gate, B_w_up, B_w_down)
```

```cpp
#include <hip/hip_runtime.h>
#include <hip/hip_bf16.h>
#include <stdint.h>

typedef unsigned short u16;
typedef short bf16x8 __attribute__((ext_vector_type(8)));
typedef float f32x4 __attribute__((ext_vector_type(4)));

constexpr int S = 2048, D = 1024, H = 32, DH = 32, W = 256, F = 3584;

__device__ __forceinline__ float bf2f(u16 v) { return __uint_as_float(((unsigned)v) << 16); }
__device__ __forceinline__ u16 f2bf(float f) {
  __bf16 b = (__bf16)f;
  return __builtin_bit_cast(u16, b);
}
__device__ __forceinline__ float rbf(float f) { return bf2f(f2bf(f)); }

__global__ __launch_bounds__(256) void ln_kernel(const u16* __restrict__ x, u16* __restrict__ xn) {
  const int row = blockIdx.x * 4 + (threadIdx.x >> 6), lane = threadIdx.x & 63;
  const u16* xr = x + (size_t)row * D;
  float v[16];
  float s = 0.f;
  for (int i = 0; i < 16; ++i) { v[i] = bf2f(xr[lane * 16 + i]); s += v[i]; }
  for (int o = 32; o; o >>= 1) s += __shfl_xor(s, o);
  const float mean = s * (1.0f / D);
  float q = 0.f;
  for (int i = 0; i < 16; ++i) { float d = v[i] - mean; q += d * d; }
  for (int o = 32; o; o >>= 1) q += __shfl_xor(q, o);
  const float mb = rbf(mean), vb = rbf(q * (1.0f / D));
  const float sd = rbf(sqrtf(rbf(vb + 1e-5f)));
  for (int i = 0; i < 16; ++i) xn[(size_t)row * D + lane * 16 + i] = f2bf(rbf(v[i] - mb) / sd);
}

__global__ __launch_bounds__(256) void gemm_kernel(const u16* __restrict__ A, int lda, const u16* __restrict__ B, int ldb,
                                                   u16* __restrict__ C, int ldc, int K) {
  __shared__ u16 As[64][40];
  __shared__ u16 Bt[64][40];
  const int tid = threadIdx.x, lane = tid & 63, w = tid >> 6, wr = w >> 1, wc = w & 1;
  const int m0 = blockIdx.y * 64, n0 = blockIdx.x * 64;
  f32x4 acc[2][2] = {};
  for (int k0 = 0; k0 < K; k0 += 32) {
    {
      const int r = tid >> 2, c = (tid & 3) * 8;
      for (int j = 0; j < 8; ++j) As[r][c + j] = A[(size_t)(m0 + r) * lda + k0 + c + j];
      const int kr = tid >> 3, nc = (tid & 7) * 8;
      for (int j = 0; j < 8; ++j) Bt[nc + j][kr] = B[(size_t)(k0 + kr) * ldb + n0 + nc + j];
    }
    __syncthreads();
    for (int i = 0; i < 2; ++i)
      for (int j = 0; j < 2; ++j) {
        bf16x8 a, b;
        for (int e = 0; e < 8; ++e) {
          a[e] = (short)As[wr * 32 + i * 16 + (lane & 15)][(lane >> 4) * 8 + e];
          b[e] = (short)Bt[wc * 32 + j * 16 + (lane & 15)][(lane >> 4) * 8 + e];
        }
        acc[i][j] = __builtin_amdgcn_mfma_f32_16x16x32_bf16(a, b, acc[i][j], 0, 0, 0);
      }
    __syncthreads();
  }
  for (int i = 0; i < 2; ++i)
    for (int j = 0; j < 2; ++j)
      for (int e = 0; e < 4; ++e)
        C[(size_t)(m0 + wr * 32 + i * 16 + (lane >> 4) * 4 + e) * ldc + n0 + wc * 32 + j * 16 + (lane & 15)] = f2bf(acc[i][j][e]);
}

__global__ void mul_kernel(const u16* __restrict__ g, const u16* __restrict__ u, u16* __restrict__ h, int n) {
  int i = blockIdx.x * blockDim.x + threadIdx.x;
  if (i < n) h[i] = f2bf(bf2f(g[i]) * bf2f(u[i]));
}

__global__ __launch_bounds__(256) void attn_kernel(const u16* __restrict__ qkv, u16* __restrict__ o) {
  const int gw = blockIdx.x * 4 + (threadIdx.x >> 6), lane = threadIdx.x & 63;
  const int q = gw / H, h = gw % H;
  float qv[DH];
  for (int d = 0; d < DH; ++d) qv[d] = bf2f(qkv[(size_t)q * 3 * D + h * DH + d]);
  float sc[4];
  float mx = -1e30f;
  for (int i = 0; i < 4; ++i) {
    const int key = q - (i * 64 + lane);
    float s = -1e30f;
    if (key >= 0) {
      float a = 0.f;
      for (int d = 0; d < DH; ++d) a += qv[d] * bf2f(qkv[(size_t)key * 3 * D + D + h * DH + d]);
      s = rbf(rbf(a) * rbf(0.17677669529663687f));
    }
    sc[i] = s;
    mx = fmaxf(mx, s);
  }
  for (int of = 32; of; of >>= 1) mx = fmaxf(mx, __shfl_xor(mx, of));
  float sum = 0.f;
  for (int i = 0; i < 4; ++i) { sc[i] = (q - (i * 64 + lane) >= 0) ? __expf(sc[i] - mx) : 0.f; sum += sc[i]; }
  for (int of = 32; of; of >>= 1) sum += __shfl_xor(sum, of);
  for (int i = 0; i < 4; ++i) sc[i] = rbf(sc[i] / sum);
  for (int d = 0; d < DH; ++d) {
    float a = 0.f;
    for (int i = 0; i < 4; ++i) {
      const int key = q - (i * 64 + lane);
      if (key >= 0) a += sc[i] * bf2f(qkv[(size_t)key * 3 * D + 2 * D + h * DH + d]);
    }
    for (int of = 32; of; of >>= 1) a += __shfl_xor(a, of);
    if (lane == 0) o[(size_t)q * D + h * DH + d] = f2bf(a);
  }
}

__global__ void final_kernel(const u16* __restrict__ x, const u16* __restrict__ a, const u16* __restrict__ b, u16* __restrict__ out, int n) {
  int i = blockIdx.x * blockDim.x + threadIdx.x;
  if (i < n) out[i] = f2bf(rbf(bf2f(x[i]) + bf2f(a[i])) + bf2f(b[i]));
}

extern "C" void kernel_launch(void* const* d_in, const int* in_sizes, int n_in, void* d_out, int out_size, void* d_ws,
                              size_t ws_size, hipStream_t stream) {
  const u16* x = (const u16*)d_in[0];
  const u16* wqkv = (const u16*)d_in[1];
  const u16* wo = (const u16*)d_in[2];
  const u16* wg = (const u16*)d_in[4];
  const u16* wu = (const u16*)d_in[5];
  const u16* wd = (const u16*)d_in[6];
  u16* ws = (u16*)d_ws;
  u16* xn = ws;
  u16* qkv = xn + (size_t)S * D;
  u16* g = qkv + (size_t)S * 3 * D;
  u16* u = g + (size_t)S * F;
  u16* h = u + (size_t)S * F;
  u16* o = h + (size_t)S * F;
  u16* ao = o + (size_t)S * D;
  u16* fo = ao + (size_t)S * D;
  ln_kernel<<<S / 4, 256, 0, stream>>>(x, xn);
  gemm_kernel<<<dim3(3 * D / 64, S / 64), 256, 0, stream>>>(xn, D, wqkv, 3 * D, qkv, 3 * D, D);
  gemm_kernel<<<dim3(F / 64, S / 64), 256, 0, stream>>>(xn, D, wg, F, g, F, D);
  gemm_kernel<<<dim3(F / 64, S / 64), 256, 0, stream>>>(xn, D, wu, F, u, F, D);
  mul_kernel<<<S * F / 256, 256, 0, stream>>>(g, u, h, S * F);
  attn_kernel<<<S * H / 4, 256, 0, stream>>>(qkv, o);
  gemm_kernel<<<dim3(D / 64, S / 64), 256, 0, stream>>>(o, D, wo, D, ao, D, D);
  gemm_kernel<<<dim3(D / 64, S / 64), 256, 0, stream>>>(h, F, wd, D, fo, D, F);
  final_kernel<<<S * D / 256, 256, 0, stream>>>(x, ao, fo, (u16*)d_out, S * D);
}
```

```cpp
#include <hip/hip_runtime.h>
#include <hip/hip_bf16.h>
#include <stdint.h>

typedef unsigned short u16;
typedef short bf16x8 __attribute__((ext_vector_type(8)));
typedef short bf16x4 __attribute__((ext_vector_type(4)));
typedef float f32x4 __attribute__((ext_vector_type(4)));
typedef float f32x16 __attribute__((ext_vector_type(16)));
typedef unsigned u32x4 __attribute__((ext_vector_type(4)));
typedef unsigned u32x2 __attribute__((ext_vector_type(2)));
typedef int i32x4 __attribute__((ext_vector_type(4)));

constexpr int S_ = 2048, D_ = 1024, F_ = 3584;
constexpr int KS2 = 144;
constexpr unsigned SY_EP = 0, SY_R = 1024, SY_F = 4096, SY_BYTES = 36864;
constexpr size_t WS_XN = 1 << 20, WS_QKV = WS_XN + (size_t)S_ * D_ * 2, WS_A2 = WS_QKV + (size_t)S_ * 3 * D_ * 2, WS_PART = WS_A2 + (size_t)S_ * KS2 * 64;
constexpr unsigned F_Q = 0, F_H = 8, F_O = 16, F_N = 24, F_X = 64;
constexpr unsigned SPIN_MAX = 1u << 21;
#define H_SPLIT 0
#define H_ORDER 0
#define H_SIG_PAIR 0
#define CODE_PF 1
#define XB16 1
#define OUT_AUX 2
#define LC_MAP 0
#define POLL_SLEEP 4

#define STAMP(k, i)
#define STAMPC(k, i)

__device__ __forceinline__ float rbf(float f) { return (float)(__bf16)f; }
__device__ __forceinline__ unsigned pk2(float lo, float hi) {
  typedef float f32x2 __attribute__((ext_vector_type(2)));
  typedef __bf16 bf2 __attribute__((ext_vector_type(2)));
  f32x2 v = {lo, hi};
  return __builtin_bit_cast(unsigned, __builtin_convertvector(v, bf2));
}
__device__ __forceinline__ i32x4 make_srd(const void* p, unsigned bytes) {
  const unsigned long long a = (unsigned long long)p;
  return (i32x4){(int)(unsigned)a, (int)(unsigned)(a >> 32) & 0xffff, (int)bytes, 0x00020000};
}
typedef unsigned long long u64;
__device__ __forceinline__ u64 ld_word(const char* sy, unsigned off) { return __hip_atomic_load((const u64*)(sy + off), __ATOMIC_RELAXED, __HIP_MEMORY_SCOPE_AGENT); }
__device__ __forceinline__ void st_word(char* sy, unsigned off, u64 v) { __hip_atomic_store((u64*)(sy + off), v, __ATOMIC_RELAXED, __HIP_MEMORY_SCOPE_AGENT); }
__device__ __forceinline__ void wait_tag(const char* sy, unsigned off, u64 tag) {
  unsigned spins = 0;
  while (!__all(ld_word(sy, off) == tag)) {
    __builtin_amdgcn_s_sleep(POLL_SLEEP);
    if (++spins > SPIN_MAX) break;
  }
  asm volatile("" ::: "memory");
}
template <int CTRL> __device__ __forceinline__ float dpp_mov(float x) { return __int_as_float(__builtin_amdgcn_update_dpp(0, __float_as_int(x), CTRL, 0xf, 0xf, true)); }
__device__ __forceinline__ float max3f(float a, float b, float c) { float r; asm("v_max3_f32 %0, %1, %2, %3" : "=v"(r) : "v"(a), "v"(b), "v"(c)); return r; }
__device__ __forceinline__ float max2f(float a, float b) { float r; asm("v_max_f32 %0, %1, %2" : "=v"(r) : "v"(a), "v"(b)); return r; }
__device__ __forceinline__ float rows_max(float x) {
  auto a = __builtin_amdgcn_permlane16_swap(__float_as_uint(x), __float_as_uint(x), false, false);
  x = max2f(__uint_as_float(a[0]), __uint_as_float(a[1]));
  auto b = __builtin_amdgcn_permlane32_swap(__float_as_uint(x), __float_as_uint(x), false, false);
  return max2f(__uint_as_float(b[0]), __uint_as_float(b[1]));
}
__device__ __forceinline__ float rows_sum(float x) {
  auto a = __builtin_amdgcn_permlane16_swap(__float_as_uint(x), __float_as_uint(x), false, false);
  x = __uint_as_float(a[0]) + __uint_as_float(a[1]);
  auto b = __builtin_amdgcn_permlane32_swap(__float_as_uint(x), __float_as_uint(x), false, false);
  return __uint_as_float(b[0]) + __uint_as_float(b[1]);
}
__device__ __forceinline__ float wave_sum(float x) {
  x += dpp_mov<0xB1>(x);
  x += dpp_mov<0x4E>(x);
  x += dpp_mov<0x141>(x);
  x += dpp_mov<0x140>(x);
  return rows_sum(x);
}
#define DRAIN_VM() asm volatile("s_waitcnt vmcnt(0)" ::: "memory")
#define BAR() do { asm volatile("s_waitcnt lgkmcnt(0)" ::: "memory"); __builtin_amdgcn_s_barrier(); asm volatile("" ::: "memory"); } while (0)
#define MFMA16(a, b, c) __builtin_amdgcn_mfma_f32_16x16x32_bf16(a, b, c, 0, 0, 0)

__global__ __launch_bounds__(512, 2) void fused_kernel(const u16* __restrict__ wqkv, const u16* __restrict__ wg,
                                                       const u16* __restrict__ wu, const u16* __restrict__ wd,
                                                       const u16* __restrict__ wo, const u16* __restrict__ x,
                                                       u16* __restrict__ out, char* __restrict__ sy) {
  u16* const xn_t = (u16*)(sy + WS_XN);
  u16* const qkv_t = (u16*)(sy + WS_QKV);
  u16* const a2_t = (u16*)(sy + WS_A2);
  float* const part = (float*)(sy + WS_PART);
  __shared__ __attribute__((aligned(1024))) char smem[163840];
  STAMP(1, 0);
  STAMPC(1, 2);
  unsigned long long pc0;
  unsigned lenA, lenB;
  asm volatile("s_getpc_b64 %0\n.Lfk_here:\n\ts_mov_b32 %1, .Lfk_mid-.Lfk_here\n\ts_mov_b32 %2, .Lfk_end-.Lfk_mid" : "=s"(pc0), "=s"(lenA), "=s"(lenB));
  const int b = blockIdx.x, xcd = b & 7, idx = b >> 3;
  const unsigned lb = (unsigned)(size_t)smem;
  const int w = __builtin_amdgcn_readfirstlane(threadIdx.x >> 6);
  const int rb = idx & 7, slot = idx >> 3;
  const int head = xcd * 4 + slot;
  const int par = w >> 2, w4 = w & 3;
  int bb = b, kh, R, lcA, lcB;
  char* const ksm = smem + 131072;
  char* const vsm = smem + 32768;
  constexpr unsigned LC = 114688;
  const __amdgpu_buffer_rsrc_t rqo = __builtin_amdgcn_make_buffer_rsrc((void*)qkv_t, 0, S_ * 3 * D_ * 2, 0x00020000);
  const __amdgpu_buffer_rsrc_t rho = __builtin_amdgcn_make_buffer_rsrc((void*)a2_t, 0, S_ * KS2 * 64, 0x00020000);
  const bool typeB = slot == 3;
  const int ta = 3 * xcd + slot;
  u64 epoch, tag;
  u32x4 hq[8];
  unsigned hoff;
  {
    const int tid = threadIdx.x, lane = tid & 63;
    u32x2 ea;
    u32x4 xa, xb;
    unsigned dummy;
    const int cs = xcd * 4 + slot;
    {
      const i32x4 rsy0 = make_srd(sy, SY_BYTES), rsx = make_srd(x, S_ * D_ * 2), rca = make_srd((const void*)pc0, lenA);
      const unsigned vz = SY_EP, vrow = (rb * 256 + cs * 8 + w) * 2048 + lane * 16;
      const unsigned vc0 = (CODE_PF && w >= 6) ? (unsigned)(((w - 6) * 128 + lane) * 128) : 0x80000000u;
      asm volatile(
          "buffer_load_dwordx4 %[xa], %[vrow], %[rsx], 0 offen\n\t"
          "buffer_load_dwordx4 %[xb], %[vrow], %[rsx], 0 offen offset:1024\n\t"
          "buffer_load_dwordx2 %[ea], %[vz], %[rsy0], 0 offen sc1\n\t"
          "s_cmp_lt_u32 %[sw], 6\n\t"
          "s_cbranch_scc1 1f\n\t"
          "buffer_load_dword %[dm], %[vc0], %[rca], 0 offen\n\t"
          "buffer_load_dword %[dm], %[vc1], %[rca], 0 offen\n\t"
          "1:"
          : [xa] "=&v"(xa), [xb] "=&v"(xb), [ea] "=&v"(ea), [dm] "=&v"(dummy)
          : [vz] "v"(vz), [rsy0] "s"(rsy0), [vrow] "v"(vrow), [rsx] "s"(rsx), [vc0] "v"(vc0), [vc1] "v"(vc0 + 64 * 128), [rca] "s"(rca), [sw] "s"(w)
          : "memory", "scc");
    }
    const int wn = w & 1, wm = w >> 1;
    const int g = lane >> 4, q = (lane >> 2) & 3, p = lane & 3;
    unsigned vA[2], vC[2][2];
    {
      const unsigned psi = (q >> 1) | ((g & 1) << 1);
#pragma unroll
      for (int h = 0; h < 2; ++h) {
        vA[h] = lb + h * 73728 + wm * 4096 + lane * 16;
#pragma unroll
        for (int t = 0; t < 2; ++t) vC[h][t] = lb + h * 73728 + 16384 + (8 * g + q) * 128 + (((2 * wn + t) ^ psi) * 32) + p * 8;
      }
    }

    i32x4 rs[6];
    unsigned vS[6], so[6], st[6], sd[6];
    const unsigned long long pxn = (unsigned long long)(xn_t + (size_t)rb * 16 * 32 * 512);
#pragma unroll
    for (int i = 0; i < 6; ++i) {
      const int e = 3 * w + (i % 3);
      const bool isx = i < 3 && e < 16;
      const int n = i < 3 ? e + 2 : e;
      const int si = n >> 2, k = 8 * (n & 3) + (lane >> 3);
      const bool isc = typeB ? si >= 2 : si >= 4, isu = typeB ? si == 1 : (si >> 1) == 1;
      const unsigned col = isc ? 64 * (typeB ? 22 + 3 * xcd + si : ta) : 64 * (typeB ? 48 + xcd : 2 * ta + (si & 1));
      const unsigned ss = isc ? 6144u : 7168u;
      const unsigned long long pw = (isc ? (unsigned long long)wqkv : (isu ? (unsigned long long)wu : (unsigned long long)wg)) + col * 2u;
      const unsigned long long pp = isx ? pxn : pw;
      rs[i] = (i32x4){(int)(unsigned)pp, (int)(unsigned)(pp >> 32) & 0xffff, (int)(isx ? 16u * 32u * 1024u : 1024u * ss - col * 2u), 0x00020000};
      const unsigned offw = k * ss + (((lane & 7) ^ ((((k >> 1) & 1) | (((k >> 3) & 1) << 1)) << 1)) * 16);
      vS[i] = isx ? (unsigned)(e * 32768 + lane * 16) : offw;
      st[i] = isx ? 1024u : 32u * ss;
      sd[i] = lb + (isx ? e * 1024 : 16384 + n * 1024);
      so[i] = 0;
    }
    const unsigned col4 = 64 * (typeB ? 26 + 3 * xcd : ta), ssub4 = 6144u;
    const unsigned long long pw4 = (unsigned long long)wqkv + col4 * 2u;
    const i32x4 rsub4 = {(int)(unsigned)pw4, (int)(unsigned)(pw4 >> 32) & 0xffff, (int)(1024u * 6144u - col4 * 2u), 0x00020000};

    STAMP(4, 4);
    {
      const unsigned vt = lane < 48 ? (unsigned)((16 + (lane & 15)) + 32 * (lane >> 4)) * ssub4 : 0x80000000u;
      asm volatile(
          "s_cmp_lt_u32 %[sw], 6\n\t"
          "s_cbranch_scc0 1f\n\t"
    "s_add_u32 m0, %[sd3], 0\n\t"
    "s_nop 0\n\t"
    "buffer_load_dwordx4 v217, %[r3], %[so3] offen nt lds\n\t"
    "s_add_u32 %[so3], %[so3], %[st3]\n\t"
    "s_add_u32 m0, %[sd4], 0\n\t"
    "s_nop 0\n\t"
    "buffer_load_dwordx4 v218, %[r4], %[so4] offen nt lds\n\t"
    "s_add_u32 %[so4], %[so4], %[st4]\n\t"
    "s_add_u32 m0, %[sd5], 0\n\t"
    "s_nop 0\n\t"
    "buffer_load_dwordx4 v219, %[r5], %[so5] offen nt lds\n\t"
    "s_add_u32 %[so5], %[so5], %[st5]\n\t"
    "s_add_u32 m0, %[sd3], 36864\n\t"
    "s_nop 0\n\t"
    "buffer_load_dwordx4 v217, %[r3], %[so3] offen nt lds\n\t"
    "s_add_u32 %[so3], %[so3], %[st3]\n\t"
    "s_add_u32 m0, %[sd4], 36864\n\t"
    "s_nop 0\n\t"
    "buffer_load_dwordx4 v218, %[r4], %[so4] offen nt lds\n\t"
    "s_add_u32 %[so4], %[so4], %[st4]\n\t"
    "s_add_u32 m0, %[sd5], 36864\n\t"
    "s_nop 0\n\t"
    "buffer_load_dwordx4 v219, %[r5], %[so5] offen nt lds\n\t"
    "s_add_u32 %[so5], %[so5], %[st5]\n\t"
    "s_add_u32 m0, %[sd3], 73728\n\t"
    "s_nop 0\n\t"
    "buffer_load_dwordx4 v217, %[r3], %[so3] offen nt lds\n\t"
    "s_add_u32 %[so3], %[so3], %[st3]\n\t"
    "s_add_u32 m0, %[sd4], 73728\n\t"
    "s_nop 0\n\t"
    "buffer_load_dwordx4 v218, %[r4], %[so4] offen nt lds\n\t"
    "s_add_u32 %[so4], %[so4], %[st4]\n\t"
    "s_add_u32 m0, %[sd5], 73728\n\t"
    "s_nop 0\n\t"
    "buffer_load_dwordx4 v219, %[r5], %[so5] offen nt lds\n\t"
    "s_add_u32 %[so5], %[so5], %[st5]\n\t"
          "s_waitcnt vmcnt(9)\n\t"
          "s_branch 2f\n\t"
          "1:\n\t"
          "buffer_load_dword %[dm], %[vt], %[rt], 0 offen\n\t"
          "s_waitcnt vmcnt(3)\n\t"
          "2:"
          : [xa] "+v"(xa), [xb] "+v"(xb), [ea] "+v"(ea), [dm] "+v"(dummy), [so3] "+&s"(so[3]), [so4] "+&s"(so[4]), [so5] "+&s"(so[5])
          : [vt] "v"(vt), [rt] "s"(rsub4), [sw] "s"(w), "{v217}"(vS[3]), "{v218}"(vS[4]), "{v219}"(vS[5]), [r3] "s"(rs[3]), [st3] "s"(st[3]), [sd3] "s"(sd[3]), [r4] "s"(rs[4]), [st4] "s"(st[4]), [sd4] "s"(sd[4]), [r5] "s"(rs[5]), [st5] "s"(st[5]), [sd5] "s"(sd[5])
          : "memory", "scc");
    }
    STAMP(4, 0);
    {
      const u64 ep = ((u64)(unsigned)__builtin_amdgcn_readfirstlane(ea[1]) << 32 | (unsigned)__builtin_amdgcn_readfirstlane(ea[0])) + 1;
      epoch = ep;
      tag = ep * 0x9E3779B97F4A7C15ull;
      if (tid == 0) st_word(sy, SY_R + b * 8, tag);
      float v[16];
#pragma unroll
      for (int i = 0; i < 4; ++i) {
        v[2 * i] = __uint_as_float(xa[i] << 16);
        v[2 * i + 1] = __uint_as_float(xa[i] & 0xffff0000u);
        v[8 + 2 * i] = __uint_as_float(xb[i] << 16);
        v[9 + 2 * i] = __uint_as_float(xb[i] & 0xffff0000u);
      }
      float s = 0.f;
#pragma unroll
      for (int i = 0; i < 16; ++i) s += v[i];
      const float mean = wave_sum(s) * (1.0f / D_);
      float qq = 0.f;
#pragma unroll
      for (int i = 0; i < 16; ++i) { const float d = v[i] - mean; qq += d * d; }
      const float var = wave_sum(qq) * (1.0f / D_);
      const float mb = rbf(mean), sd = rbf(sqrtf(rbf(rbf(var) + 1e-5f)));
      const float rs_ = 1.0f / sd;
      u32x4 oa, ob;
#pragma unroll
      for (int i = 0; i < 4; ++i) {
        oa[i] = pk2(rbf(v[2 * i] - mb) * rs_, rbf(v[2 * i + 1] - mb) * rs_);
        ob[i] = pk2(rbf(v[8 + 2 * i] - mb) * rs_, rbf(v[9 + 2 * i] - mb) * rs_);
      }
      u32x4* tile = (u32x4*)(smem + 3 * 36864);
      tile[lane * 8 + w] = oa;
      tile[(lane + 64) * 8 + w] = ob;
      BAR();
      STAMP(4, 1);
      const __amdgpu_buffer_rsrc_t rxn = __builtin_amdgcn_make_buffer_rsrc((void*)xn_t, 0, S_ * D_ * 2, 0x00020000);
      const int mt = rb * 16 + (cs >> 1), c0 = (cs & 1) * 8;
#pragma unroll
      for (int i = 0; i < 2; ++i) {
        const int ix = tid + i * 512, kc = ix >> 3, tok = ix & 7;
        __builtin_amdgcn_raw_buffer_store_b128(tile[ix], rxn, (unsigned)((mt * 32 + (kc >> 2)) * 64 + (kc & 3) * 16 + c0 + tok) * 16u, 0, 16);
      }
      asm volatile("s_waitcnt vmcnt(0)" : "+v"(dummy) :: "memory");
      BAR();
      STAMP(4, 2);
      if (tid == 0) st_word(sy, SY_F + b * 128 + F_N, tag);
      if (w == 0) {
        const int pc = lane & 31;
        wait_tag(sy, SY_F + ((pc >> 2) + 8 * ((pc & 3) * 8 + rb)) * 128 + F_N, tag);
      }
      BAR();
      STAMP(4, 3);
    }
    if (w < 6) {
      asm volatile(
    "s_add_u32 m0, %[sd0], 0\n\t"
    "s_nop 0\n\t"
    "buffer_load_dwordx4 v214, %[r0], %[so0] offen sc1 lds\n\t"
    "s_add_u32 %[so0], %[so0], %[st0]\n\t"
    "s_add_u32 m0, %[sd1], 0\n\t"
    "s_nop 0\n\t"
    "buffer_load_dwordx4 v215, %[r1], %[so1] offen sc1 lds\n\t"
    "s_add_u32 %[so1], %[so1], %[st1]\n\t"
    "s_add_u32 m0, %[sd2], 0\n\t"
    "s_nop 0\n\t"
    "buffer_load_dwordx4 v216, %[r2], %[so2] offen sc1 lds\n\t"
    "s_add_u32 %[so2], %[so2], %[st2]\n\t"
    "s_add_u32 m0, %[sd0], 36864\n\t"
    "s_nop 0\n\t"
    "buffer_load_dwordx4 v214, %[r0], %[so0] offen sc1 lds\n\t"
    "s_add_u32 %[so0], %[so0], %[st0]\n\t"
    "s_add_u32 m0, %[sd1], 36864\n\t"
    "s_nop 0\n\t"
    "buffer_load_dwordx4 v215, %[r1], %[so1] offen sc1 lds\n\t"
    "s_add_u32 %[so1], %[so1], %[st1]\n\t"
    "s_add_u32 m0, %[sd2], 36864\n\t"
    "s_nop 0\n\t"
    "buffer_load_dwordx4 v216, %[r2], %[so2] offen sc1 lds\n\t"
    "s_add_u32 %[so2], %[so2], %[st2]\n\t"
    "s_add_u32 m0, %[sd0], 73728\n\t"
    "s_nop 0\n\t"
    "buffer_load_dwordx4 v214, %[r0], %[so0] offen sc1 lds\n\t"
    "s_add_u32 %[so0], %[so0], %[st0]\n\t"
    "s_add_u32 m0, %[sd1], 73728\n\t"
    "s_nop 0\n\t"
    "buffer_load_dwordx4 v215, %[r1], %[so1] offen sc1 lds\n\t"
    "s_add_u32 %[so1], %[so1], %[st1]\n\t"
    "s_add_u32 m0, %[sd2], 73728\n\t"
    "s_nop 0\n\t"
    "buffer_load_dwordx4 v216, %[r2], %[so2] offen sc1 lds\n\t"
    "s_add_u32 %[so2], %[so2], %[st2]\n\t"
          : [so0] "+&s"(so[0]), [so1] "+&s"(so[1]), [so2] "+&s"(so[2])
          : "{v214}"(vS[0]), "{v215}"(vS[1]), "{v216}"(vS[2]), [r0] "s"(rs[0]), [st0] "s"(st[0]), [sd0] "s"(sd[0]), [r1] "s"(rs[1]), [st1] "s"(st[1]), [sd1] "s"(sd[1]), [r2] "s"(rs[2]), [st2] "s"(st[2]), [sd2] "s"(sd[2])
          : "memory", "scc");
    }
    f32x16 acc0, acc1, acc2, acc3, acc4, acc5, acc6, acc7, acc8, acc9;
    if (w < 6) {
      unsigned cnt;
      asm volatile(
    "s_waitcnt vmcnt(6)\n\t"
    "s_barrier\n\t"
    "ds_read_b128 v[160:163], v208 offset:0\n\t"
    "ds_read_b128 v[164:167], v208 offset:1024\n\t"
    "ds_read_b128 v[168:171], v208 offset:2048\n\t"
    "ds_read_b128 v[172:175], v208 offset:3072\n\t"
    "ds_read_b64_tr_b16 v[192:193], v209 offset:0\n\t"
    "ds_read_b64_tr_b16 v[194:195], v209 offset:512\n\t"
    "ds_read_b64_tr_b16 v[196:197], v210 offset:0\n\t"
    "ds_read_b64_tr_b16 v[198:199], v210 offset:512\n\t"
    "ds_read_b64_tr_b16 v[200:201], v209 offset:4096\n\t"
    "ds_read_b64_tr_b16 v[202:203], v209 offset:4608\n\t"
    "s_waitcnt lgkmcnt(4)\n\t"
    "v_mfma_f32_16x16x32_bf16 v[0:3], v[192:195], v[160:163], 0\n\t"
    "ds_read_b64_tr_b16 v[204:205], v210 offset:4096\n\t"
    "v_mfma_f32_16x16x32_bf16 v[4:7], v[192:195], v[164:167], 0\n\t"
    "ds_read_b64_tr_b16 v[206:207], v210 offset:4608\n\t"
    "v_mfma_f32_16x16x32_bf16 v[8:11], v[192:195], v[168:171], 0\n\t"
    "v_mfma_f32_16x16x32_bf16 v[12:15], v[192:195], v[172:175], 0\n\t"
    "s_waitcnt lgkmcnt(4)\n\t"
    "v_mfma_f32_16x16x32_bf16 v[16:19], v[196:199], v[160:163], 0\n\t"
    "ds_read_b64_tr_b16 v[192:193], v209 offset:8192\n\t"
    "v_mfma_f32_16x16x32_bf16 v[20:23], v[196:199], v[164:167], 0\n\t"
    "ds_read_b64_tr_b16 v[194:195], v209 offset:8704\n\t"
    "v_mfma_f32_16x16x32_bf16 v[24:27], v[196:199], v[168:171], 0\n\t"
    "v_mfma_f32_16x16x32_bf16 v[28:31], v[196:199], v[172:175], 0\n\t"
    "s_waitcnt lgkmcnt(4)\n\t"
    "v_mfma_f32_16x16x32_bf16 v[32:35], v[200:203], v[160:163], 0\n\t"
    "ds_read_b64_tr_b16 v[196:197], v210 offset:8192\n\t"
    "v_mfma_f32_16x16x32_bf16 v[36:39], v[200:203], v[164:167], 0\n\t"
    "ds_read_b64_tr_b16 v[198:199], v210 offset:8704\n\t"
    "v_mfma_f32_16x16x32_bf16 v[40:43], v[200:203], v[168:171], 0\n\t"
    "v_mfma_f32_16x16x32_bf16 v[44:47], v[200:203], v[172:175], 0\n\t"
    "s_waitcnt lgkmcnt(4)\n\t"
    "v_mfma_f32_16x16x32_bf16 v[48:51], v[204:207], v[160:163], 0\n\t"
    "ds_read_b64_tr_b16 v[200:201], v209 offset:12288\n\t"
    "v_mfma_f32_16x16x32_bf16 v[52:55], v[204:207], v[164:167], 0\n\t"
    "ds_read_b64_tr_b16 v[202:203], v209 offset:12800\n\t"
    "v_mfma_f32_16x16x32_bf16 v[56:59], v[204:207], v[168:171], 0\n\t"
    "v_mfma_f32_16x16x32_bf16 v[60:63], v[204:207], v[172:175], 0\n\t"
    "s_waitcnt lgkmcnt(4)\n\t"
    "v_mfma_f32_16x16x32_bf16 v[64:67], v[192:195], v[160:163], 0\n\t"
    "ds_read_b64_tr_b16 v[204:205], v210 offset:12288\n\t"
    "v_mfma_f32_16x16x32_bf16 v[68:71], v[192:195], v[164:167], 0\n\t"
    "ds_read_b64_tr_b16 v[206:207], v210 offset:12800\n\t"
    "v_mfma_f32_16x16x32_bf16 v[72:75], v[192:195], v[168:171], 0\n\t"
    "v_mfma_f32_16x16x32_bf16 v[76:79], v[192:195], v[172:175], 0\n\t"
    "s_waitcnt lgkmcnt(4)\n\t"
    "v_mfma_f32_16x16x32_bf16 v[80:83], v[196:199], v[160:163], 0\n\t"
    "ds_read_b64_tr_b16 v[192:193], v209 offset:16384\n\t"
    "v_mfma_f32_16x16x32_bf16 v[84:87], v[196:199], v[164:167], 0\n\t"
    "ds_read_b64_tr_b16 v[194:195], v209 offset:16896\n\t"
    "v_mfma_f32_16x16x32_bf16 v[88:91], v[196:199], v[168:171], 0\n\t"
    "v_mfma_f32_16x16x32_bf16 v[92:95], v[196:199], v[172:175], 0\n\t"
    "s_waitcnt vmcnt(3)\n\t"
    "s_barrier\n\t"
    "s_waitcnt lgkmcnt(4)\n\t"
    "v_mfma_f32_16x16x32_bf16 v[96:99], v[200:203], v[160:163], 0\n\t"
    "ds_read_b64_tr_b16 v[196:197], v210 offset:16384\n\t"
    "v_mfma_f32_16x16x32_bf16 v[100:103], v[200:203], v[164:167], 0\n\t"
    "ds_read_b64_tr_b16 v[198:199], v210 offset:16896\n\t"
    "v_mfma_f32_16x16x32_bf16 v[104:107], v[200:203], v[168:171], 0\n\t"
    "ds_read_b128 v[176:179], v208 offset:36864\n\t"
    "s_add_u32 m0, %[sd0], 110592\n\t"
    "v_mfma_f32_16x16x32_bf16 v[108:111], v[200:203], v[172:175], 0\n\t"
    "buffer_load_dwordx4 v214, %[r0], %[so0] offen sc1 lds\n\t"
    "s_add_u32 %[so0], %[so0], %[st0]\n\t"
    "s_waitcnt lgkmcnt(5)\n\t"
    "v_mfma_f32_16x16x32_bf16 v[112:115], v[204:207], v[160:163], 0\n\t"
    "ds_read_b64_tr_b16 v[200:201], v209 offset:36864\n\t"
    "v_mfma_f32_16x16x32_bf16 v[116:119], v[204:207], v[164:167], 0\n\t"
    "ds_read_b64_tr_b16 v[202:203], v209 offset:37376\n\t"
    "v_mfma_f32_16x16x32_bf16 v[120:123], v[204:207], v[168:171], 0\n\t"
    "ds_read_b128 v[180:183], v208 offset:37888\n\t"
    "s_add_u32 m0, %[sd1], 110592\n\t"
    "v_mfma_f32_16x16x32_bf16 v[124:127], v[204:207], v[172:175], 0\n\t"
    "buffer_load_dwordx4 v215, %[r1], %[so1] offen sc1 lds\n\t"
    "s_add_u32 %[so1], %[so1], %[st1]\n\t"
    "s_waitcnt lgkmcnt(6)\n\t"
    "v_mfma_f32_16x16x32_bf16 v[128:131], v[192:195], v[160:163], 0\n\t"
    "ds_read_b64_tr_b16 v[204:205], v210 offset:36864\n\t"
    "v_mfma_f32_16x16x32_bf16 v[132:135], v[192:195], v[164:167], 0\n\t"
    "ds_read_b64_tr_b16 v[206:207], v210 offset:37376\n\t"
    "v_mfma_f32_16x16x32_bf16 v[136:139], v[192:195], v[168:171], 0\n\t"
    "ds_read_b128 v[184:187], v208 offset:38912\n\t"
    "s_add_u32 m0, %[sd2], 110592\n\t"
    "v_mfma_f32_16x16x32_bf16 v[140:143], v[192:195], v[172:175], 0\n\t"
    "buffer_load_dwordx4 v216, %[r2], %[so2] offen sc1 lds\n\t"
    "s_add_u32 %[so2], %[so2], %[st2]\n\t"
    "s_waitcnt lgkmcnt(7)\n\t"
    "v_mfma_f32_16x16x32_bf16 v[144:147], v[196:199], v[160:163], 0\n\t"
    "ds_read_b64_tr_b16 v[192:193], v209 offset:40960\n\t"
    "v_mfma_f32_16x16x32_bf16 v[148:151], v[196:199], v[164:167], 0\n\t"
    "ds_read_b64_tr_b16 v[194:195], v209 offset:41472\n\t"
    "v_mfma_f32_16x16x32_bf16 v[152:155], v[196:199], v[168:171], 0\n\t"
    "ds_read_b128 v[188:191], v208 offset:39936\n\t"
    "s_add_u32 m0, %[sd3], 110592\n\t"
    "v_mfma_f32_16x16x32_bf16 v[156:159], v[196:199], v[172:175], 0\n\t"
    "buffer_load_dwordx4 v217, %[r3], %[so3] offen nt lds\n\t"
    "s_add_u32 %[so3], %[so3], %[st3]\n\t"
    "s_waitcnt lgkmcnt(7)\n\t"
    "v_mfma_f32_16x16x32_bf16 v[0:3], v[200:203], v[176:179], v[0:3]\n\t"
    "ds_read_b64_tr_b16 v[196:197], v210 offset:40960\n\t"
    "s_waitcnt lgkmcnt(7)\n\t"
    "v_mfma_f32_16x16x32_bf16 v[4:7], v[200:203], v[180:183], v[4:7]\n\t"
    "ds_read_b64_tr_b16 v[198:199], v210 offset:41472\n\t"
    "s_waitcnt lgkmcnt(5)\n\t"
    "v_mfma_f32_16x16x32_bf16 v[8:11], v[200:203], v[184:187], v[8:11]\n\t"
    "s_add_u32 m0, %[sd4], 110592\n\t"
    "s_waitcnt lgkmcnt(2)\n\t"
    "v_mfma_f32_16x16x32_bf16 v[12:15], v[200:203], v[188:191], v[12:15]\n\t"
    "buffer_load_dwordx4 v218, %[r4], %[so4] offen nt lds\n\t"
    "s_add_u32 %[so4], %[so4], %[st4]\n\t"
    "v_mfma_f32_16x16x32_bf16 v[16:19], v[204:207], v[176:179], v[16:19]\n\t"
    "ds_read_b64_tr_b16 v[200:201], v209 offset:45056\n\t"
    "v_mfma_f32_16x16x32_bf16 v[20:23], v[204:207], v[180:183], v[20:23]\n\t"
    "ds_read_b64_tr_b16 v[202:203], v209 offset:45568\n\t"
    "v_mfma_f32_16x16x32_bf16 v[24:27], v[204:207], v[184:187], v[24:27]\n\t"
    "s_add_u32 m0, %[sd5], 110592\n\t"
    "v_mfma_f32_16x16x32_bf16 v[28:31], v[204:207], v[188:191], v[28:31]\n\t"
    "buffer_load_dwordx4 v219, %[r5], %[so5] offen nt lds\n\t"
    "s_add_u32 %[so5], %[so5], %[st5]\n\t"
    "v_mfma_f32_16x16x32_bf16 v[32:35], v[192:195], v[176:179], v[32:35]\n\t"
    "ds_read_b64_tr_b16 v[204:205], v210 offset:45056\n\t"
    "v_mfma_f32_16x16x32_bf16 v[36:39], v[192:195], v[180:183], v[36:39]\n\t"
    "ds_read_b64_tr_b16 v[206:207], v210 offset:45568\n\t"
    "v_mfma_f32_16x16x32_bf16 v[40:43], v[192:195], v[184:187], v[40:43]\n\t"
    "v_mfma_f32_16x16x32_bf16 v[44:47], v[192:195], v[188:191], v[44:47]\n\t"
    "s_waitcnt lgkmcnt(4)\n\t"
    "v_mfma_f32_16x16x32_bf16 v[48:51], v[196:199], v[176:179], v[48:51]\n\t"
    "ds_read_b64_tr_b16 v[192:193], v209 offset:49152\n\t"
    "v_mfma_f32_16x16x32_bf16 v[52:55], v[196:199], v[180:183], v[52:55]\n\t"
    "ds_read_b64_tr_b16 v[194:195], v209 offset:49664\n\t"
    "v_mfma_f32_16x16x32_bf16 v[56:59], v[196:199], v[184:187], v[56:59]\n\t"
    "v_mfma_f32_16x16x32_bf16 v[60:63], v[196:199], v[188:191], v[60:63]\n\t"
    "s_waitcnt lgkmcnt(4)\n\t"
    "v_mfma_f32_16x16x32_bf16 v[64:67], v[200:203], v[176:179], v[64:67]\n\t"
    "ds_read_b64_tr_b16 v[196:197], v210 offset:49152\n\t"
    "v_mfma_f32_16x16x32_bf16 v[68:71], v[200:203], v[180:183], v[68:71]\n\t"
    "ds_read_b64_tr_b16 v[198:199], v210 offset:49664\n\t"
    "v_mfma_f32_16x16x32_bf16 v[72:75], v[200:203], v[184:187], v[72:75]\n\t"
    "v_mfma_f32_16x16x32_bf16 v[76:79], v[200:203], v[188:191], v[76:79]\n\t"
    "s_waitcnt lgkmcnt(4)\n\t"
    "v_mfma_f32_16x16x32_bf16 v[80:83], v[204:207], v[176:179], v[80:83]\n\t"
    "ds_read_b64_tr_b16 v[200:201], v209 offset:53248\n\t"
    "v_mfma_f32_16x16x32_bf16 v[84:87], v[204:207], v[180:183], v[84:87]\n\t"
    "ds_read_b64_tr_b16 v[202:203], v209 offset:53760\n\t"
    "v_mfma_f32_16x16x32_bf16 v[88:91], v[204:207], v[184:187], v[88:91]\n\t"
    "v_mfma_f32_16x16x32_bf16 v[92:95], v[204:207], v[188:191], v[92:95]\n\t"
    "s_waitcnt vmcnt(6)\n\t"
    "s_barrier\n\t"
    "s_waitcnt lgkmcnt(4)\n\t"
    "v_mfma_f32_16x16x32_bf16 v[96:99], v[192:195], v[176:179], v[96:99]\n\t"
    "ds_read_b64_tr_b16 v[204:205], v210 offset:53248\n\t"
    "v_mfma_f32_16x16x32_bf16 v[100:103], v[192:195], v[180:183], v[100:103]\n\t"
    "ds_read_b64_tr_b16 v[206:207], v210 offset:53760\n\t"
    "v_mfma_f32_16x16x32_bf16 v[104:107], v[192:195], v[184:187], v[104:107]\n\t"
    "ds_read_b128 v[160:163], v211 offset:0\n\t"
    "s_add_u32 m0, %[sd0], 0\n\t"
    "v_mfma_f32_16x16x32_bf16 v[108:111], v[192:195], v[188:191], v[108:111]\n\t"
    "buffer_load_dwordx4 v214, %[r0], %[so0] offen sc1 lds\n\t"
    "s_add_u32 %[so0], %[so0], %[st0]\n\t"
    "s_waitcnt lgkmcnt(5)\n\t"
    "v_mfma_f32_16x16x32_bf16 v[112:115], v[196:199], v[176:179], v[112:115]\n\t"
    "ds_read_b64_tr_b16 v[192:193], v212 offset:0\n\t"
    "v_mfma_f32_16x16x32_bf16 v[116:119], v[196:199], v[180:183], v[116:119]\n\t"
    "ds_read_b64_tr_b16 v[194:195], v212 offset:512\n\t"
    "v_mfma_f32_16x16x32_bf16 v[120:123], v[196:199], v[184:187], v[120:123]\n\t"
    "ds_read_b128 v[164:167], v211 offset:1024\n\t"
    "s_add_u32 m0, %[sd1], 0\n\t"
    "v_mfma_f32_16x16x32_bf16 v[124:127], v[196:199], v[188:191], v[124:127]\n\t"
    "buffer_load_dwordx4 v215, %[r1], %[so1] offen sc1 lds\n\t"
    "s_add_u32 %[so1], %[so1], %[st1]\n\t"
    "s_waitcnt lgkmcnt(6)\n\t"
    "v_mfma_f32_16x16x32_bf16 v[128:131], v[200:203], v[176:179], v[128:131]\n\t"
    "ds_read_b64_tr_b16 v[196:197], v213 offset:0\n\t"
    "v_mfma_f32_16x16x32_bf16 v[132:135], v[200:203], v[180:183], v[132:135]\n\t"
    "ds_read_b64_tr_b16 v[198:199], v213 offset:512\n\t"
    "v_mfma_f32_16x16x32_bf16 v[136:139], v[200:203], v[184:187], v[136:139]\n\t"
    "ds_read_b128 v[168:171], v211 offset:2048\n\t"
    "s_add_u32 m0, %[sd2], 0\n\t"
    "v_mfma_f32_16x16x32_bf16 v[140:143], v[200:203], v[188:191], v[140:143]\n\t"
    "buffer_load_dwordx4 v216, %[r2], %[so2] offen sc1 lds\n\t"
    "s_add_u32 %[so2], %[so2], %[st2]\n\t"
    "s_waitcnt lgkmcnt(7)\n\t"
    "v_mfma_f32_16x16x32_bf16 v[144:147], v[204:207], v[176:179], v[144:147]\n\t"
    "ds_read_b64_tr_b16 v[200:201], v212 offset:4096\n\t"
    "v_mfma_f32_16x16x32_bf16 v[148:151], v[204:207], v[180:183], v[148:151]\n\t"
    "ds_read_b64_tr_b16 v[202:203], v212 offset:4608\n\t"
    "v_mfma_f32_16x16x32_bf16 v[152:155], v[204:207], v[184:187], v[152:155]\n\t"
    "ds_read_b128 v[172:175], v211 offset:3072\n\t"
    "s_add_u32 m0, %[sd3], 0\n\t"
    "v_mfma_f32_16x16x32_bf16 v[156:159], v[204:207], v[188:191], v[156:159]\n\t"
    "buffer_load_dwordx4 v217, %[r3], %[so3] offen nt lds\n\t"
    "s_add_u32 %[so3], %[so3], %[st3]\n\t"
    "s_waitcnt lgkmcnt(7)\n\t"
    "v_mfma_f32_16x16x32_bf16 v[0:3], v[192:195], v[160:163], v[0:3]\n\t"
    "ds_read_b64_tr_b16 v[204:205], v213 offset:4096\n\t"
    "s_waitcnt lgkmcnt(7)\n\t"
    "v_mfma_f32_16x16x32_bf16 v[4:7], v[192:195], v[164:167], v[4:7]\n\t"
    "ds_read_b64_tr_b16 v[206:207], v213 offset:4608\n\t"
    "s_waitcnt lgkmcnt(5)\n\t"
    "v_mfma_f32_16x16x32_bf16 v[8:11], v[192:195], v[168:171], v[8:11]\n\t"
    "s_add_u32 m0, %[sd4], 0\n\t"
    "s_waitcnt lgkmcnt(2)\n\t"
    "v_mfma_f32_16x16x32_bf16 v[12:15], v[192:195], v[172:175], v[12:15]\n\t"
    "buffer_load_dwordx4 v218, %[r4], %[so4] offen nt lds\n\t"
    "s_add_u32 %[so4], %[so4], %[st4]\n\t"
    "v_mfma_f32_16x16x32_bf16 v[16:19], v[196:199], v[160:163], v[16:19]\n\t"
    "ds_read_b64_tr_b16 v[192:193], v212 offset:8192\n\t"
    "v_mfma_f32_16x16x32_bf16 v[20:23], v[196:199], v[164:167], v[20:23]\n\t"
    "ds_read_b64_tr_b16 v[194:195], v212 offset:8704\n\t"
    "v_mfma_f32_16x16x32_bf16 v[24:27], v[196:199], v[168:171], v[24:27]\n\t"
    "s_add_u32 m0, %[sd5], 0\n\t"
    "v_mfma_f32_16x16x32_bf16 v[28:31], v[196:199], v[172:175], v[28:31]\n\t"
    "buffer_load_dwordx4 v219, %[r5], %[so5] offen nt lds\n\t"
    "s_add_u32 %[so5], %[so5], %[st5]\n\t"
    "v_mfma_f32_16x16x32_bf16 v[32:35], v[200:203], v[160:163], v[32:35]\n\t"
    "ds_read_b64_tr_b16 v[196:197], v213 offset:8192\n\t"
    "v_mfma_f32_16x16x32_bf16 v[36:39], v[200:203], v[164:167], v[36:39]\n\t"
    "ds_read_b64_tr_b16 v[198:199], v213 offset:8704\n\t"
    "v_mfma_f32_16x16x32_bf16 v[40:43], v[200:203], v[168:171], v[40:43]\n\t"
    "v_mfma_f32_16x16x32_bf16 v[44:47], v[200:203], v[172:175], v[44:47]\n\t"
    "s_waitcnt lgkmcnt(4)\n\t"
    "v_mfma_f32_16x16x32_bf16 v[48:51], v[204:207], v[160:163], v[48:51]\n\t"
    "ds_read_b64_tr_b16 v[200:201], v212 offset:12288\n\t"
    "v_mfma_f32_16x16x32_bf16 v[52:55], v[204:207], v[164:167], v[52:55]\n\t"
    "ds_read_b64_tr_b16 v[202:203], v212 offset:12800\n\t"
    "v_mfma_f32_16x16x32_bf16 v[56:59], v[204:207], v[168:171], v[56:59]\n\t"
    "v_mfma_f32_16x16x32_bf16 v[60:63], v[204:207], v[172:175], v[60:63]\n\t"
    "s_waitcnt lgkmcnt(4)\n\t"
    "v_mfma_f32_16x16x32_bf16 v[64:67], v[192:195], v[160:163], v[64:67]\n\t"
    "ds_read_b64_tr_b16 v[204:205], v213 offset:12288\n\t"
    "v_mfma_f32_16x16x32_bf16 v[68:71], v[192:195], v[164:167], v[68:71]\n\t"
    "ds_read_b64_tr_b16 v[206:207], v213 offset:12800\n\t"
    "v_mfma_f32_16x16x32_bf16 v[72:75], v[192:195], v[168:171], v[72:75]\n\t"
    "v_mfma_f32_16x16x32_bf16 v[76:79], v[192:195], v[172:175], v[76:79]\n\t"
    "s_waitcnt lgkmcnt(4)\n\t"
    "v_mfma_f32_16x16x32_bf16 v[80:83], v[196:199], v[160:163], v[80:83]\n\t"
    "ds_read_b64_tr_b16 v[192:193], v212 offset:16384\n\t"
    "v_mfma_f32_16x16x32_bf16 v[84:87], v[196:199], v[164:167], v[84:87]\n\t"
    "ds_read_b64_tr_b16 v[194:195], v212 offset:16896\n\t"
    "v_mfma_f32_16x16x32_bf16 v[88:91], v[196:199], v[168:171], v[88:91]\n\t"
    "v_mfma_f32_16x16x32_bf16 v[92:95], v[196:199], v[172:175], v[92:95]\n\t"
    "s_waitcnt vmcnt(6)\n\t"
    "s_barrier\n\t"
    "s_waitcnt lgkmcnt(4)\n\t"
    "v_mfma_f32_16x16x32_bf16 v[96:99], v[200:203], v[160:163], v[96:99]\n\t"
    "ds_read_b64_tr_b16 v[196:197], v213 offset:16384\n\t"
    "v_mfma_f32_16x16x32_bf16 v[100:103], v[200:203], v[164:167], v[100:103]\n\t"
    "ds_read_b64_tr_b16 v[198:199], v213 offset:16896\n\t"
    "v_mfma_f32_16x16x32_bf16 v[104:107], v[200:203], v[168:171], v[104:107]\n\t"
    "ds_read_b128 v[176:179], v211 offset:36864\n\t"
    "s_add_u32 m0, %[sd0], 36864\n\t"
    "v_mfma_f32_16x16x32_bf16 v[108:111], v[200:203], v[172:175], v[108:111]\n\t"
    "buffer_load_dwordx4 v214, %[r0], %[so0] offen sc1 lds\n\t"
    "s_add_u32 %[so0], %[so0], %[st0]\n\t"
    "s_waitcnt lgkmcnt(5)\n\t"
    "v_mfma_f32_16x16x32_bf16 v[112:115], v[204:207], v[160:163], v[112:115]\n\t"
    "ds_read_b64_tr_b16 v[200:201], v212 offset:36864\n\t"
    "v_mfma_f32_16x16x32_bf16 v[116:119], v[204:207], v[164:167], v[116:119]\n\t"
    "ds_read_b64_tr_b16 v[202:203], v212 offset:37376\n\t"
    "v_mfma_f32_16x16x32_bf16 v[120:123], v[204:207], v[168:171], v[120:123]\n\t"
    "ds_read_b128 v[180:183], v211 offset:37888\n\t"
    "s_add_u32 m0, %[sd1], 36864\n\t"
    "v_mfma_f32_16x16x32_bf16 v[124:127], v[204:207], v[172:175], v[124:127]\n\t"
    "buffer_load_dwordx4 v215, %[r1], %[so1] offen sc1 lds\n\t"
    "s_add_u32 %[so1], %[so1], %[st1]\n\t"
    "s_waitcnt lgkmcnt(6)\n\t"
    "v_mfma_f32_16x16x32_bf16 v[128:131], v[192:195], v[160:163], v[128:131]\n\t"
    "ds_read_b64_tr_b16 v[204:205], v213 offset:36864\n\t"
    "v_mfma_f32_16x16x32_bf16 v[132:135], v[192:195], v[164:167], v[132:135]\n\t"
    "ds_read_b64_tr_b16 v[206:207], v213 offset:37376\n\t"
    "v_mfma_f32_16x16x32_bf16 v[136:139], v[192:195], v[168:171], v[136:139]\n\t"
    "ds_read_b128 v[184:187], v211 offset:38912\n\t"
    "s_add_u32 m0, %[sd2], 36864\n\t"
    "v_mfma_f32_16x16x32_bf16 v[140:143], v[192:195], v[172:175], v[140:143]\n\t"
    "buffer_load_dwordx4 v216, %[r2], %[so2] offen sc1 lds\n\t"
    "s_add_u32 %[so2], %[so2], %[st2]\n\t"
    "s_waitcnt lgkmcnt(7)\n\t"
    "v_mfma_f32_16x16x32_bf16 v[144:147], v[196:199], v[160:163], v[144:147]\n\t"
    "ds_read_b64_tr_b16 v[192:193], v212 offset:40960\n\t"
    "v_mfma_f32_16x16x32_bf16 v[148:151], v[196:199], v[164:167], v[148:151]\n\t"
    "ds_read_b64_tr_b16 v[194:195], v212 offset:41472\n\t"
    "v_mfma_f32_16x16x32_bf16 v[152:155], v[196:199], v[168:171], v[152:155]\n\t"
    "ds_read_b128 v[188:191], v211 offset:39936\n\t"
    "s_add_u32 m0, %[sd3], 36864\n\t"
    "v_mfma_f32_16x16x32_bf16 v[156:159], v[196:199], v[172:175], v[156:159]\n\t"
    "buffer_load_dwordx4 v217, %[r3], %[so3] offen nt lds\n\t"
    "s_add_u32 %[so3], %[so3], %[st3]\n\t"
    "s_waitcnt lgkmcnt(7)\n\t"
    "v_mfma_f32_16x16x32_bf16 v[0:3], v[200:203], v[176:179], v[0:3]\n\t"
    "ds_read_b64_tr_b16 v[196:197], v213 offset:40960\n\t"
    "s_waitcnt lgkmcnt(7)\n\t"
    "v_mfma_f32_16x16x32_bf16 v[4:7], v[200:203], v[180:183], v[4:7]\n\t"
    "ds_read_b64_tr_b16 v[198:199], v213 offset:41472\n\t"
    "s_waitcnt lgkmcnt(5)\n\t"
    "v_mfma_f32_16x16x32_bf16 v[8:11], v[200:203], v[184:187], v[8:11]\n\t"
    "s_add_u32 m0, %[sd4], 36864\n\t"
    "s_waitcnt lgkmcnt(2)\n\t"
    "v_mfma_f32_16x16x32_bf16 v[12:15], v[200:203], v[188:191], v[12:15]\n\t"
    "buffer_load_dwordx4 v218, %[r4], %[so4] offen nt lds\n\t"
    "s_add_u32 %[so4], %[so4], %[st4]\n\t"
    "v_mfma_f32_16x16x32_bf16 v[16:19], v[204:207], v[176:179], v[16:19]\n\t"
    "ds_read_b64_tr_b16 v[200:201], v212 offset:45056\n\t"
    "v_mfma_f32_16x16x32_bf16 v[20:23], v[204:207], v[180:183], v[20:23]\n\t"
    "ds_read_b64_tr_b16 v[202:203], v212 offset:45568\n\t"
    "v_mfma_f32_16x16x32_bf16 v[24:27], v[204:207], v[184:187], v[24:27]\n\t"
    "s_add_u32 m0, %[sd5], 36864\n\t"
    "v_mfma_f32_16x16x32_bf16 v[28:31], v[204:207], v[188:191], v[28:31]\n\t"
    "buffer_load_dwordx4 v219, %[r5], %[so5] offen nt lds\n\t"
    "s_add_u32 %[so5], %[so5], %[st5]\n\t"
    "v_mfma_f32_16x16x32_bf16 v[32:35], v[192:195], v[176:179], v[32:35]\n\t"
    "ds_read_b64_tr_b16 v[204:205], v213 offset:45056\n\t"
    "v_mfma_f32_16x16x32_bf16 v[36:39], v[192:195], v[180:183], v[36:39]\n\t"
    "ds_read_b64_tr_b16 v[206:207], v213 offset:45568\n\t"
    "v_mfma_f32_16x16x32_bf16 v[40:43], v[192:195], v[184:187], v[40:43]\n\t"
    "v_mfma_f32_16x16x32_bf16 v[44:47], v[192:195], v[188:191], v[44:47]\n\t"
    "s_waitcnt lgkmcnt(4)\n\t"
    "v_mfma_f32_16x16x32_bf16 v[48:51], v[196:199], v[176:179], v[48:51]\n\t"
    "ds_read_b64_tr_b16 v[192:193], v212 offset:49152\n\t"
    "v_mfma_f32_16x16x32_bf16 v[52:55], v[196:199], v[180:183], v[52:55]\n\t"
    "ds_read_b64_tr_b16 v[194:195], v212 offset:49664\n\t"
    "v_mfma_f32_16x16x32_bf16 v[56:59], v[196:199], v[184:187], v[56:59]\n\t"
    "v_mfma_f32_16x16x32_bf16 v[60:63], v[196:199], v[188:191], v[60:63]\n\t"
    "s_waitcnt lgkmcnt(4)\n\t"
    "v_mfma_f32_16x16x32_bf16 v[64:67], v[200:203], v[176:179], v[64:67]\n\t"
    "ds_read_b64_tr_b16 v[196:197], v213 offset:49152\n\t"
    "v_mfma_f32_16x16x32_bf16 v[68:71], v[200:203], v[180:183], v[68:71]\n\t"
    "ds_read_b64_tr_b16 v[198:199], v213 offset:49664\n\t"
    "v_mfma_f32_16x16x32_bf16 v[72:75], v[200:203], v[184:187], v[72:75]\n\t"
    "v_mfma_f32_16x16x32_bf16 v[76:79], v[200:203], v[188:191], v[76:79]\n\t"
    "s_waitcnt lgkmcnt(4)\n\t"
    "v_mfma_f32_16x16x32_bf16 v[80:83], v[204:207], v[176:179], v[80:83]\n\t"
    "ds_read_b64_tr_b16 v[200:201], v212 offset:53248\n\t"
    "v_mfma_f32_16x16x32_bf16 v[84:87], v[204:207], v[180:183], v[84:87]\n\t"
    "ds_read_b64_tr_b16 v[202:203], v212 offset:53760\n\t"
    "v_mfma_f32_16x16x32_bf16 v[88:91], v[204:207], v[184:187], v[88:91]\n\t"
    "v_mfma_f32_16x16x32_bf16 v[92:95], v[204:207], v[188:191], v[92:95]\n\t"
    "s_waitcnt vmcnt(6)\n\t"
    "s_barrier\n\t"
    "s_waitcnt lgkmcnt(4)\n\t"
    "v_mfma_f32_16x16x32_bf16 v[96:99], v[192:195], v[176:179], v[96:99]\n\t"
    "ds_read_b64_tr_b16 v[204:205], v213 offset:53248\n\t"
    "v_mfma_f32_16x16x32_bf16 v[100:103], v[192:195], v[180:183], v[100:103]\n\t"
    "ds_read_b64_tr_b16 v[206:207], v213 offset:53760\n\t"
    "v_mfma_f32_16x16x32_bf16 v[104:107], v[192:195], v[184:187], v[104:107]\n\t"
    "ds_read_b128 v[160:163], v208 offset:0\n\t"
    "s_add_u32 m0, %[sd0], 73728\n\t"
    "v_mfma_f32_16x16x32_bf16 v[108:111], v[192:195], v[188:191], v[108:111]\n\t"
    "buffer_load_dwordx4 v214, %[r0], %[so0] offen sc1 lds\n\t"
    "s_add_u32 %[so0], %[so0], %[st0]\n\t"
    "s_waitcnt lgkmcnt(5)\n\t"
    "v_mfma_f32_16x16x32_bf16 v[112:115], v[196:199], v[176:179], v[112:115]\n\t"
    "ds_read_b64_tr_b16 v[192:193], v209 offset:0\n\t"
    "v_mfma_f32_16x16x32_bf16 v[116:119], v[196:199], v[180:183], v[116:119]\n\t"
    "ds_read_b64_tr_b16 v[194:195], v209 offset:512\n\t"
    "v_mfma_f32_16x16x32_bf16 v[120:123], v[196:199], v[184:187], v[120:123]\n\t"
    "ds_read_b128 v[164:167], v208 offset:1024\n\t"
    "s_add_u32 m0, %[sd1], 73728\n\t"
    "v_mfma_f32_16x16x32_bf16 v[124:127], v[196:199], v[188:191], v[124:127]\n\t"
    "buffer_load_dwordx4 v215, %[r1], %[so1] offen sc1 lds\n\t"
    "s_add_u32 %[so1], %[so1], %[st1]\n\t"
    "s_waitcnt lgkmcnt(6)\n\t"
    "v_mfma_f32_16x16x32_bf16 v[128:131], v[200:203], v[176:179], v[128:131]\n\t"
    "ds_read_b64_tr_b16 v[196:197], v210 offset:0\n\t"
    "v_mfma_f32_16x16x32_bf16 v[132:135], v[200:203], v[180:183], v[132:135]\n\t"
    "ds_read_b64_tr_b16 v[198:199], v210 offset:512\n\t"
    "v_mfma_f32_16x16x32_bf16 v[136:139], v[200:203], v[184:187], v[136:139]\n\t"
    "ds_read_b128 v[168:171], v208 offset:2048\n\t"
    "s_add_u32 m0, %[sd2], 73728\n\t"
    "v_mfma_f32_16x16x32_bf16 v[140:143], v[200:203], v[188:191], v[140:143]\n\t"
    "buffer_load_dwordx4 v216, %[r2], %[so2] offen sc1 lds\n\t"
    "s_add_u32 %[so2], %[so2], %[st2]\n\t"
    "s_waitcnt lgkmcnt(7)\n\t"
    "v_mfma_f32_16x16x32_bf16 v[144:147], v[204:207], v[176:179], v[144:147]\n\t"
    "ds_read_b64_tr_b16 v[200:201], v209 offset:4096\n\t"
    "v_mfma_f32_16x16x32_bf16 v[148:151], v[204:207], v[180:183], v[148:151]\n\t"
    "ds_read_b64_tr_b16 v[202:203], v209 offset:4608\n\t"
    "v_mfma_f32_16x16x32_bf16 v[152:155], v[204:207], v[184:187], v[152:155]\n\t"
    "ds_read_b128 v[172:175], v208 offset:3072\n\t"
    "s_add_u32 m0, %[sd3], 73728\n\t"
    "v_mfma_f32_16x16x32_bf16 v[156:159], v[204:207], v[188:191], v[156:159]\n\t"
    "buffer_load_dwordx4 v217, %[r3], %[so3] offen nt lds\n\t"
    "s_add_u32 %[so3], %[so3], %[st3]\n\t"
    "s_mov_b32 %[cnt], 6\n\t"
    "2:\n\t"
    "s_waitcnt lgkmcnt(7)\n\t"
    "v_mfma_f32_16x16x32_bf16 v[0:3], v[192:195], v[160:163], v[0:3]\n\t"
    "ds_read_b64_tr_b16 v[204:205], v210 offset:4096\n\t"
    "s_waitcnt lgkmcnt(7)\n\t"
    "v_mfma_f32_16x16x32_bf16 v[4:7], v[192:195], v[164:167], v[4:7]\n\t"
    "ds_read_b64_tr_b16 v[206:207], v210 offset:4608\n\t"
    "s_waitcnt lgkmcnt(5)\n\t"
    "v_mfma_f32_16x16x32_bf16 v[8:11], v[192:195], v[168:171], v[8:11]\n\t"
    "s_add_u32 m0, %[sd4], 73728\n\t"
    "s_waitcnt lgkmcnt(2)\n\t"
    "v_mfma_f32_16x16x32_bf16 v[12:15], v[192:195], v[172:175], v[12:15]\n\t"
    "buffer_load_dwordx4 v218, %[r4], %[so4] offen nt lds\n\t"
    "s_add_u32 %[so4], %[so4], %[st4]\n\t"
    "v_mfma_f32_16x16x32_bf16 v[16:19], v[196:199], v[160:163], v[16:19]\n\t"
    "ds_read_b64_tr_b16 v[192:193], v209 offset:8192\n\t"
    "v_mfma_f32_16x16x32_bf16 v[20:23], v[196:199], v[164:167], v[20:23]\n\t"
    "ds_read_b64_tr_b16 v[194:195], v209 offset:8704\n\t"
    "v_mfma_f32_16x16x32_bf16 v[24:27], v[196:199], v[168:171], v[24:27]\n\t"
    "s_add_u32 m0, %[sd5], 73728\n\t"
    "v_mfma_f32_16x16x32_bf16 v[28:31], v[196:199], v[172:175], v[28:31]\n\t"
    "buffer_load_dwordx4 v219, %[r5], %[so5] offen nt lds\n\t"
    "s_add_u32 %[so5], %[so5], %[st5]\n\t"
    "v_mfma_f32_16x16x32_bf16 v[32:35], v[200:203], v[160:163], v[32:35]\n\t"
    "ds_read_b64_tr_b16 v[196:197], v210 offset:8192\n\t"
    "v_mfma_f32_16x16x32_bf16 v[36:39], v[200:203], v[164:167], v[36:39]\n\t"
    "ds_read_b64_tr_b16 v[198:199], v210 offset:8704\n\t"
    "v_mfma_f32_16x16x32_bf16 v[40:43], v[200:203], v[168:171], v[40:43]\n\t"
    "v_mfma_f32_16x16x32_bf16 v[44:47], v[200:203], v[172:175], v[44:47]\n\t"
    "s_waitcnt lgkmcnt(4)\n\t"
    "v_mfma_f32_16x16x32_bf16 v[48:51], v[204:207], v[160:163], v[48:51]\n\t"
    "ds_read_b64_tr_b16 v[200:201], v209 offset:12288\n\t"
    "v_mfma_f32_16x16x32_bf16 v[52:55], v[204:207], v[164:167], v[52:55]\n\t"
    "ds_read_b64_tr_b16 v[202:203], v209 offset:12800\n\t"
    "v_mfma_f32_16x16x32_bf16 v[56:59], v[204:207], v[168:171], v[56:59]\n\t"
    "v_mfma_f32_16x16x32_bf16 v[60:63], v[204:207], v[172:175], v[60:63]\n\t"
    "s_waitcnt lgkmcnt(4)\n\t"
    "v_mfma_f32_16x16x32_bf16 v[64:67], v[192:195], v[160:163], v[64:67]\n\t"
    "ds_read_b64_tr_b16 v[204:205], v210 offset:12288\n\t"
    "v_mfma_f32_16x16x32_bf16 v[68:71], v[192:195], v[164:167], v[68:71]\n\t"
    "ds_read_b64_tr_b16 v[206:207], v210 offset:12800\n\t"
    "v_mfma_f32_16x16x32_bf16 v[72:75], v[192:195], v[168:171], v[72:75]\n\t"
    "v_mfma_f32_16x16x32_bf16 v[76:79], v[192:195], v[172:175], v[76:79]\n\t"
    "s_waitcnt lgkmcnt(4)\n\t"
    "v_mfma_f32_16x16x32_bf16 v[80:83], v[196:199], v[160:163], v[80:83]\n\t"
    "ds_read_b64_tr_b16 v[192:193], v209 offset:16384\n\t"
    "v_mfma_f32_16x16x32_bf16 v[84:87], v[196:199], v[164:167], v[84:87]\n\t"
    "ds_read_b64_tr_b16 v[194:195], v209 offset:16896\n\t"
    "v_mfma_f32_16x16x32_bf16 v[88:91], v[196:199], v[168:171], v[88:91]\n\t"
    "v_mfma_f32_16x16x32_bf16 v[92:95], v[196:199], v[172:175], v[92:95]\n\t"
    "s_waitcnt vmcnt(6)\n\t"
    "s_barrier\n\t"
    "s_waitcnt lgkmcnt(4)\n\t"
    "v_mfma_f32_16x16x32_bf16 v[96:99], v[200:203], v[160:163], v[96:99]\n\t"
    "ds_read_b64_tr_b16 v[196:197], v210 offset:16384\n\t"
    "v_mfma_f32_16x16x32_bf16 v[100:103], v[200:203], v[164:167], v[100:103]\n\t"
    "ds_read_b64_tr_b16 v[198:199], v210 offset:16896\n\t"
    "v_mfma_f32_16x16x32_bf16 v[104:107], v[200:203], v[168:171], v[104:107]\n\t"
    "ds_read_b128 v[176:179], v208 offset:36864\n\t"
    "s_add_u32 m0, %[sd0], 110592\n\t"
    "v_mfma_f32_16x16x32_bf16 v[108:111], v[200:203], v[172:175], v[108:111]\n\t"
    "buffer_load_dwordx4 v214, %[r0], %[so0] offen sc1 lds\n\t"
    "s_add_u32 %[so0], %[so0], %[st0]\n\t"
    "s_waitcnt lgkmcnt(5)\n\t"
    "v_mfma_f32_16x16x32_bf16 v[112:115], v[204:207], v[160:163], v[112:115]\n\t"
    "ds_read_b64_tr_b16 v[200:201], v209 offset:36864\n\t"
    "v_mfma_f32_16x16x32_bf16 v[116:119], v[204:207], v[164:167], v[116:119]\n\t"
    "ds_read_b64_tr_b16 v[202:203], v209 offset:37376\n\t"
    "v_mfma_f32_16x16x32_bf16 v[120:123], v[204:207], v[168:171], v[120:123]\n\t"
    "ds_read_b128 v[180:183], v208 offset:37888\n\t"
    "s_add_u32 m0, %[sd1], 110592\n\t"
    "v_mfma_f32_16x16x32_bf16 v[124:127], v[204:207], v[172:175], v[124:127]\n\t"
    "buffer_load_dwordx4 v215, %[r1], %[so1] offen sc1 lds\n\t"
    "s_add_u32 %[so1], %[so1], %[st1]\n\t"
    "s_waitcnt lgkmcnt(6)\n\t"
    "v_mfma_f32_16x16x32_bf16 v[128:131], v[192:195], v[160:163], v[128:131]\n\t"
    "ds_read_b64_tr_b16 v[204:205], v210 offset:36864\n\t"
    "v_mfma_f32_16x16x32_bf16 v[132:135], v[192:195], v[164:167], v[132:135]\n\t"
    "ds_read_b64_tr_b16 v[206:207], v210 offset:37376\n\t"
    "v_mfma_f32_16x16x32_bf16 v[136:139], v[192:195], v[168:171], v[136:139]\n\t"
    "ds_read_b128 v[184:187], v208 offset:38912\n\t"
    "s_add_u32 m0, %[sd2], 110592\n\t"
    "v_mfma_f32_16x16x32_bf16 v[140:143], v[192:195], v[172:175], v[140:143]\n\t"
    "buffer_load_dwordx4 v216, %[r2], %[so2] offen sc1 lds\n\t"
    "s_add_u32 %[so2], %[so2], %[st2]\n\t"
    "s_waitcnt lgkmcnt(7)\n\t"
    "v_mfma_f32_16x16x32_bf16 v[144:147], v[196:199], v[160:163], v[144:147]\n\t"
    "ds_read_b64_tr_b16 v[192:193], v209 offset:40960\n\t"
    "v_mfma_f32_16x16x32_bf16 v[148:151], v[196:199], v[164:167], v[148:151]\n\t"
    "ds_read_b64_tr_b16 v[194:195], v209 offset:41472\n\t"
    "v_mfma_f32_16x16x32_bf16 v[152:155], v[196:199], v[168:171], v[152:155]\n\t"
    "ds_read_b128 v[188:191], v208 offset:39936\n\t"
    "s_add_u32 m0, %[sd3], 110592\n\t"
    "v_mfma_f32_16x16x32_bf16 v[156:159], v[196:199], v[172:175], v[156:159]\n\t"
    "buffer_load_dwordx4 v217, %[r3], %[so3] offen nt lds\n\t"
    "s_add_u32 %[so3], %[so3], %[st3]\n\t"
    "s_waitcnt lgkmcnt(7)\n\t"
    "v_mfma_f32_16x16x32_bf16 v[0:3], v[200:203], v[176:179], v[0:3]\n\t"
    "ds_read_b64_tr_b16 v[196:197], v210 offset:40960\n\t"
    "s_waitcnt lgkmcnt(7)\n\t"
    "v_mfma_f32_16x16x32_bf16 v[4:7], v[200:203], v[180:183], v[4:7]\n\t"
    "ds_read_b64_tr_b16 v[198:199], v210 offset:41472\n\t"
    "s_waitcnt lgkmcnt(5)\n\t"
    "v_mfma_f32_16x16x32_bf16 v[8:11], v[200:203], v[184:187], v[8:11]\n\t"
    "s_add_u32 m0, %[sd4], 110592\n\t"
    "s_waitcnt lgkmcnt(2)\n\t"
    "v_mfma_f32_16x16x32_bf16 v[12:15], v[200:203], v[188:191], v[12:15]\n\t"
    "buffer_load_dwordx4 v218, %[r4], %[so4] offen nt lds\n\t"
    "s_add_u32 %[so4], %[so4], %[st4]\n\t"
    "v_mfma_f32_16x16x32_bf16 v[16:19], v[204:207], v[176:179], v[16:19]\n\t"
    "ds_read_b64_tr_b16 v[200:201], v209 offset:45056\n\t"
    "v_mfma_f32_16x16x32_bf16 v[20:23], v[204:207], v[180:183], v[20:23]\n\t"
    "ds_read_b64_tr_b16 v[202:203], v209 offset:45568\n\t"
    "v_mfma_f32_16x16x32_bf16 v[24:27], v[204:207], v[184:187], v[24:27]\n\t"
    "s_add_u32 m0, %[sd5], 110592\n\t"
    "v_mfma_f32_16x16x32_bf16 v[28:31], v[204:207], v[188:191], v[28:31]\n\t"
    "buffer_load_dwordx4 v219, %[r5], %[so5] offen nt lds\n\t"
    "s_add_u32 %[so5], %[so5], %[st5]\n\t"
    "v_mfma_f32_16x16x32_bf16 v[32:35], v[192:195], v[176:179], v[32:35]\n\t"
    "ds_read_b64_tr_b16 v[204:205], v210 offset:45056\n\t"
    "v_mfma_f32_16x16x32_bf16 v[36:39], v[192:195], v[180:183], v[36:39]\n\t"
    "ds_read_b64_tr_b16 v[206:207], v210 offset:45568\n\t"
    "v_mfma_f32_16x16x32_bf16 v[40:43], v[192:195], v[184:187], v[40:43]\n\t"
    "v_mfma_f32_16x16x32_bf16 v[44:47], v[192:195], v[188:191], v[44:47]\n\t"
    "s_waitcnt lgkmcnt(4)\n\t"
    "v_mfma_f32_16x16x32_bf16 v[48:51], v[196:199], v[176:179], v[48:51]\n\t"
    "ds_read_b64_tr_b16 v[192:193], v209 offset:49152\n\t"
    "v_mfma_f32_16x16x32_bf16 v[52:55], v[196:199], v[180:183], v[52:55]\n\t"
    "ds_read_b64_tr_b16 v[194:195], v209 offset:49664\n\t"
    "v_mfma_f32_16x16x32_bf16 v[56:59], v[196:199], v[184:187], v[56:59]\n\t"
    "v_mfma_f32_16x16x32_bf16 v[60:63], v[196:199], v[188:191], v[60:63]\n\t"
    "s_waitcnt lgkmcnt(4)\n\t"
    "v_mfma_f32_16x16x32_bf16 v[64:67], v[200:203], v[176:179], v[64:67]\n\t"
    "ds_read_b64_tr_b16 v[196:197], v210 offset:49152\n\t"
    "v_mfma_f32_16x16x32_bf16 v[68:71], v[200:203], v[180:183], v[68:71]\n\t"
    "ds_read_b64_tr_b16 v[198:199], v210 offset:49664\n\t"
    "v_mfma_f32_16x16x32_bf16 v[72:75], v[200:203], v[184:187], v[72:75]\n\t"
    "v_mfma_f32_16x16x32_bf16 v[76:79], v[200:203], v[188:191], v[76:79]\n\t"
    "s_waitcnt lgkmcnt(4)\n\t"
    "v_mfma_f32_16x16x32_bf16 v[80:83], v[204:207], v[176:179], v[80:83]\n\t"
    "ds_read_b64_tr_b16 v[200:201], v209 offset:53248\n\t"
    "v_mfma_f32_16x16x32_bf16 v[84:87], v[204:207], v[180:183], v[84:87]\n\t"
    "ds_read_b64_tr_b16 v[202:203], v209 offset:53760\n\t"
    "v_mfma_f32_16x16x32_bf16 v[88:91], v[204:207], v[184:187], v[88:91]\n\t"
    "v_mfma_f32_16x16x32_bf16 v[92:95], v[204:207], v[188:191], v[92:95]\n\t"
    "s_waitcnt vmcnt(6)\n\t"
    "s_barrier\n\t"
    "s_waitcnt lgkmcnt(4)\n\t"
    "v_mfma_f32_16x16x32_bf16 v[96:99], v[192:195], v[176:179], v[96:99]\n\t"
    "ds_read_b64_tr_b16 v[204:205], v210 offset:53248\n\t"
    "v_mfma_f32_16x16x32_bf16 v[100:103], v[192:195], v[180:183], v[100:103]\n\t"
    "ds_read_b64_tr_b16 v[206:207], v210 offset:53760\n\t"
    "v_mfma_f32_16x16x32_bf16 v[104:107], v[192:195], v[184:187], v[104:107]\n\t"
    "ds_read_b128 v[160:163], v211 offset:0\n\t"
    "s_add_u32 m0, %[sd0], 0\n\t"
    "v_mfma_f32_16x16x32_bf16 v[108:111], v[192:195], v[188:191], v[108:111]\n\t"
    "buffer_load_dwordx4 v214, %[r0], %[so0] offen sc1 lds\n\t"
    "s_add_u32 %[so0], %[so0], %[st0]\n\t"
    "s_waitcnt lgkmcnt(5)\n\t"
    "v_mfma_f32_16x16x32_bf16 v[112:115], v[196:199], v[176:179], v[112:115]\n\t"
    "ds_read_b64_tr_b16 v[192:193], v212 offset:0\n\t"
    "v_mfma_f32_16x16x32_bf16 v[116:119], v[196:199], v[180:183], v[116:119]\n\t"
    "ds_read_b64_tr_b16 v[194:195], v212 offset:512\n\t"
    "v_mfma_f32_16x16x32_bf16 v[120:123], v[196:199], v[184:187], v[120:123]\n\t"
    "ds_read_b128 v[164:167], v211 offset:1024\n\t"
    "s_add_u32 m0, %[sd1], 0\n\t"
    "v_mfma_f32_16x16x32_bf16 v[124:127], v[196:199], v[188:191], v[124:127]\n\t"
    "buffer_load_dwordx4 v215, %[r1], %[so1] offen sc1 lds\n\t"
    "s_add_u32 %[so1], %[so1], %[st1]\n\t"
    "s_waitcnt lgkmcnt(6)\n\t"
    "v_mfma_f32_16x16x32_bf16 v[128:131], v[200:203], v[176:179], v[128:131]\n\t"
    "ds_read_b64_tr_b16 v[196:197], v213 offset:0\n\t"
    "v_mfma_f32_16x16x32_bf16 v[132:135], v[200:203], v[180:183], v[132:135]\n\t"
    "ds_read_b64_tr_b16 v[198:199], v213 offset:512\n\t"
    "v_mfma_f32_16x16x32_bf16 v[136:139], v[200:203], v[184:187], v[136:139]\n\t"
    "ds_read_b128 v[168:171], v211 offset:2048\n\t"
    "s_add_u32 m0, %[sd2], 0\n\t"
    "v_mfma_f32_16x16x32_bf16 v[140:143], v[200:203], v[188:191], v[140:143]\n\t"
    "buffer_load_dwordx4 v216, %[r2], %[so2] offen sc1 lds\n\t"
    "s_add_u32 %[so2], %[so2], %[st2]\n\t"
    "s_waitcnt lgkmcnt(7)\n\t"
    "v_mfma_f32_16x16x32_bf16 v[144:147], v[204:207], v[176:179], v[144:147]\n\t"
    "ds_read_b64_tr_b16 v[200:201], v212 offset:4096\n\t"
    "v_mfma_f32_16x16x32_bf16 v[148:151], v[204:207], v[180:183], v[148:151]\n\t"
    "ds_read_b64_tr_b16 v[202:203], v212 offset:4608\n\t"
    "v_mfma_f32_16x16x32_bf16 v[152:155], v[204:207], v[184:187], v[152:155]\n\t"
    "ds_read_b128 v[172:175], v211 offset:3072\n\t"
    "s_add_u32 m0, %[sd3], 0\n\t"
    "v_mfma_f32_16x16x32_bf16 v[156:159], v[204:207], v[188:191], v[156:159]\n\t"
    "buffer_load_dwordx4 v217, %[r3], %[so3] offen nt lds\n\t"
    "s_add_u32 %[so3], %[so3], %[st3]\n\t"
    "s_waitcnt lgkmcnt(7)\n\t"
    "v_mfma_f32_16x16x32_bf16 v[0:3], v[192:195], v[160:163], v[0:3]\n\t"
    "ds_read_b64_tr_b16 v[204:205], v213 offset:4096\n\t"
    "s_waitcnt lgkmcnt(7)\n\t"
    "v_mfma_f32_16x16x32_bf16 v[4:7], v[192:195], v[164:167], v[4:7]\n\t"
    "ds_read_b64_tr_b16 v[206:207], v213 offset:4608\n\t"
    "s_waitcnt lgkmcnt(5)\n\t"
    "v_mfma_f32_16x16x32_bf16 v[8:11], v[192:195], v[168:171], v[8:11]\n\t"
    "s_add_u32 m0, %[sd4], 0\n\t"
    "s_waitcnt lgkmcnt(2)\n\t"
    "v_mfma_f32_16x16x32_bf16 v[12:15], v[192:195], v[172:175], v[12:15]\n\t"
    "buffer_load_dwordx4 v218, %[r4], %[so4] offen nt lds\n\t"
    "s_add_u32 %[so4], %[so4], %[st4]\n\t"
    "v_mfma_f32_16x16x32_bf16 v[16:19], v[196:199], v[160:163], v[16:19]\n\t"
    "ds_read_b64_tr_b16 v[192:193], v212 offset:8192\n\t"
    "v_mfma_f32_16x16x32_bf16 v[20:23], v[196:199], v[164:167], v[20:23]\n\t"
    "ds_read_b64_tr_b16 v[194:195], v212 offset:8704\n\t"
    "v_mfma_f32_16x16x32_bf16 v[24:27], v[196:199], v[168:171], v[24:27]\n\t"
    "s_add_u32 m0, %[sd5], 0\n\t"
    "v_mfma_f32_16x16x32_bf16 v[28:31], v[196:199], v[172:175], v[28:31]\n\t"
    "buffer_load_dwordx4 v219, %[r5], %[so5] offen nt lds\n\t"
    "s_add_u32 %[so5], %[so5], %[st5]\n\t"
    "v_mfma_f32_16x16x32_bf16 v[32:35], v[200:203], v[160:163], v[32:35]\n\t"
    "ds_read_b64_tr_b16 v[196:197], v213 offset:8192\n\t"
    "v_mfma_f32_16x16x32_bf16 v[36:39], v[200:203], v[164:167], v[36:39]\n\t"
    "ds_read_b64_tr_b16 v[198:199], v213 offset:8704\n\t"
    "v_mfma_f32_16x16x32_bf16 v[40:43], v[200:203], v[168:171], v[40:43]\n\t"
    "v_mfma_f32_16x16x32_bf16 v[44:47], v[200:203], v[172:175], v[44:47]\n\t"
    "s_waitcnt lgkmcnt(4)\n\t"
    "v_mfma_f32_16x16x32_bf16 v[48:51], v[204:207], v[160:163], v[48:51]\n\t"
    "ds_read_b64_tr_b16 v[200:201], v212 offset:12288\n\t"
    "v_mfma_f32_16x16x32_bf16 v[52:55], v[204:207], v[164:167], v[52:55]\n\t"
    "ds_read_b64_tr_b16 v[202:203], v212 offset:12800\n\t"
    "v_mfma_f32_16x16x32_bf16 v[56:59], v[204:207], v[168:171], v[56:59]\n\t"
    "v_mfma_f32_16x16x32_bf16 v[60:63], v[204:207], v[172:175], v[60:63]\n\t"
    "s_waitcnt lgkmcnt(4)\n\t"
    "v_mfma_f32_16x16x32_bf16 v[64:67], v[192:195], v[160:163], v[64:67]\n\t"
    "ds_read_b64_tr_b16 v[204:205], v213 offset:12288\n\t"
    "v_mfma_f32_16x16x32_bf16 v[68:71], v[192:195], v[164:167], v[68:71]\n\t"
    "ds_read_b64_tr_b16 v[206:207], v213 offset:12800\n\t"
    "v_mfma_f32_16x16x32_bf16 v[72:75], v[192:195], v[168:171], v[72:75]\n\t"
    "v_mfma_f32_16x16x32_bf16 v[76:79], v[192:195], v[172:175], v[76:79]\n\t"
    "s_waitcnt lgkmcnt(4)\n\t"
    "v_mfma_f32_16x16x32_bf16 v[80:83], v[196:199], v[160:163], v[80:83]\n\t"
    "ds_read_b64_tr_b16 v[192:193], v212 offset:16384\n\t"
    "v_mfma_f32_16x16x32_bf16 v[84:87], v[196:199], v[164:167], v[84:87]\n\t"
    "ds_read_b64_tr_b16 v[194:195], v212 offset:16896\n\t"
    "v_mfma_f32_16x16x32_bf16 v[88:91], v[196:199], v[168:171], v[88:91]\n\t"
    "v_mfma_f32_16x16x32_bf16 v[92:95], v[196:199], v[172:175], v[92:95]\n\t"
    "s_waitcnt vmcnt(6)\n\t"
    "s_barrier\n\t"
    "s_waitcnt lgkmcnt(4)\n\t"
    "v_mfma_f32_16x16x32_bf16 v[96:99], v[200:203], v[160:163], v[96:99]\n\t"
    "ds_read_b64_tr_b16 v[196:197], v213 offset:16384\n\t"
    "v_mfma_f32_16x16x32_bf16 v[100:103], v[200:203], v[164:167], v[100:103]\n\t"
    "ds_read_b64_tr_b16 v[198:199], v213 offset:16896\n\t"
    "v_mfma_f32_16x16x32_bf16 v[104:107], v[200:203], v[168:171], v[104:107]\n\t"
    "ds_read_b128 v[176:179], v211 offset:36864\n\t"
    "s_add_u32 m0, %[sd0], 36864\n\t"
    "v_mfma_f32_16x16x32_bf16 v[108:111], v[200:203], v[172:175], v[108:111]\n\t"
    "buffer_load_dwordx4 v214, %[r0], %[so0] offen sc1 lds\n\t"
    "s_add_u32 %[so0], %[so0], %[st0]\n\t"
    "s_waitcnt lgkmcnt(5)\n\t"
    "v_mfma_f32_16x16x32_bf16 v[112:115], v[204:207], v[160:163], v[112:115]\n\t"
    "ds_read_b64_tr_b16 v[200:201], v212 offset:36864\n\t"
    "v_mfma_f32_16x16x32_bf16 v[116:119], v[204:207], v[164:167], v[116:119]\n\t"
    "ds_read_b64_tr_b16 v[202:203], v212 offset:37376\n\t"
    "v_mfma_f32_16x16x32_bf16 v[120:123], v[204:207], v[168:171], v[120:123]\n\t"
    "ds_read_b128 v[180:183], v211 offset:37888\n\t"
    "s_add_u32 m0, %[sd1], 36864\n\t"
    "v_mfma_f32_16x16x32_bf16 v[124:127], v[204:207], v[172:175], v[124:127]\n\t"
    "buffer_load_dwordx4 v215, %[r1], %[so1] offen sc1 lds\n\t"
    "s_add_u32 %[so1], %[so1], %[st1]\n\t"
    "s_waitcnt lgkmcnt(6)\n\t"
    "v_mfma_f32_16x16x32_bf16 v[128:131], v[192:195], v[160:163], v[128:131]\n\t"
    "ds_read_b64_tr_b16 v[204:205], v213 offset:36864\n\t"
    "v_mfma_f32_16x16x32_bf16 v[132:135], v[192:195], v[164:167], v[132:135]\n\t"
    "ds_read_b64_tr_b16 v[206:207], v213 offset:37376\n\t"
    "v_mfma_f32_16x16x32_bf16 v[136:139], v[192:195], v[168:171], v[136:139]\n\t"
    "ds_read_b128 v[184:187], v211 offset:38912\n\t"
    "s_add_u32 m0, %[sd2], 36864\n\t"
    "v_mfma_f32_16x16x32_bf16 v[140:143], v[192:195], v[172:175], v[140:143]\n\t"
    "buffer_load_dwordx4 v216, %[r2], %[so2] offen sc1 lds\n\t"
    "s_add_u32 %[so2], %[so2], %[st2]\n\t"
    "s_waitcnt lgkmcnt(7)\n\t"
    "v_mfma_f32_16x16x32_bf16 v[144:147], v[196:199], v[160:163], v[144:147]\n\t"
    "ds_read_b64_tr_b16 v[192:193], v212 offset:40960\n\t"
    "v_mfma_f32_16x16x32_bf16 v[148:151], v[196:199], v[164:167], v[148:151]\n\t"
    "ds_read_b64_tr_b16 v[194:195], v212 offset:41472\n\t"
    "v_mfma_f32_16x16x32_bf16 v[152:155], v[196:199], v[168:171], v[152:155]\n\t"
    "ds_read_b128 v[188:191], v211 offset:39936\n\t"
    "s_add_u32 m0, %[sd3], 36864\n\t"
    "v_mfma_f32_16x16x32_bf16 v[156:159], v[196:199], v[172:175], v[156:159]\n\t"
    "buffer_load_dwordx4 v217, %[r3], %[so3] offen nt lds\n\t"
    "s_add_u32 %[so3], %[so3], %[st3]\n\t"
    "s_waitcnt lgkmcnt(7)\n\t"
    "v_mfma_f32_16x16x32_bf16 v[0:3], v[200:203], v[176:179], v[0:3]\n\t"
    "ds_read_b64_tr_b16 v[196:197], v213 offset:40960\n\t"
    "s_waitcnt lgkmcnt(7)\n\t"
    "v_mfma_f32_16x16x32_bf16 v[4:7], v[200:203], v[180:183], v[4:7]\n\t"
    "ds_read_b64_tr_b16 v[198:199], v213 offset:41472\n\t"
    "s_waitcnt lgkmcnt(5)\n\t"
    "v_mfma_f32_16x16x32_bf16 v[8:11], v[200:203], v[184:187], v[8:11]\n\t"
    "s_add_u32 m0, %[sd4], 36864\n\t"
    "s_waitcnt lgkmcnt(2)\n\t"
    "v_mfma_f32_16x16x32_bf16 v[12:15], v[200:203], v[188:191], v[12:15]\n\t"
    "buffer_load_dwordx4 v218, %[r4], %[so4] offen nt lds\n\t"
    "s_add_u32 %[so4], %[so4], %[st4]\n\t"
    "v_mfma_f32_16x16x32_bf16 v[16:19], v[204:207], v[176:179], v[16:19]\n\t"
    "ds_read_b64_tr_b16 v[200:201], v212 offset:45056\n\t"
    "v_mfma_f32_16x16x32_bf16 v[20:23], v[204:207], v[180:183], v[20:23]\n\t"
    "ds_read_b64_tr_b16 v[202:203], v212 offset:45568\n\t"
    "v_mfma_f32_16x16x32_bf16 v[24:27], v[204:207], v[184:187], v[24:27]\n\t"
    "s_add_u32 m0, %[sd5], 36864\n\t"
    "v_mfma_f32_16x16x32_bf16 v[28:31], v[204:207], v[188:191], v[28:31]\n\t"
    "buffer_load_dwordx4 v219, %[r5], %[so5] offen nt lds\n\t"
    "s_add_u32 %[so5], %[so5], %[st5]\n\t"
    "v_mfma_f32_16x16x32_bf16 v[32:35], v[192:195], v[176:179], v[32:35]\n\t"
    "ds_read_b64_tr_b16 v[204:205], v213 offset:45056\n\t"
    "v_mfma_f32_16x16x32_bf16 v[36:39], v[192:195], v[180:183], v[36:39]\n\t"
    "ds_read_b64_tr_b16 v[206:207], v213 offset:45568\n\t"
    "v_mfma_f32_16x16x32_bf16 v[40:43], v[192:195], v[184:187], v[40:43]\n\t"
    "v_mfma_f32_16x16x32_bf16 v[44:47], v[192:195], v[188:191], v[44:47]\n\t"
    "s_waitcnt lgkmcnt(4)\n\t"
    "v_mfma_f32_16x16x32_bf16 v[48:51], v[196:199], v[176:179], v[48:51]\n\t"
    "ds_read_b64_tr_b16 v[192:193], v212 offset:49152\n\t"
    "v_mfma_f32_16x16x32_bf16 v[52:55], v[196:199], v[180:183], v[52:55]\n\t"
    "ds_read_b64_tr_b16 v[194:195], v212 offset:49664\n\t"
    "v_mfma_f32_16x16x32_bf16 v[56:59], v[196:199], v[184:187], v[56:59]\n\t"
    "v_mfma_f32_16x16x32_bf16 v[60:63], v[196:199], v[188:191], v[60:63]\n\t"
    "s_waitcnt lgkmcnt(4)\n\t"
    "v_mfma_f32_16x16x32_bf16 v[64:67], v[200:203], v[176:179], v[64:67]\n\t"
    "ds_read_b64_tr_b16 v[196:197], v213 offset:49152\n\t"
    "v_mfma_f32_16x16x32_bf16 v[68:71], v[200:203], v[180:183], v[68:71]\n\t"
    "ds_read_b64_tr_b16 v[198:199], v213 offset:49664\n\t"
    "v_mfma_f32_16x16x32_bf16 v[72:75], v[200:203], v[184:187], v[72:75]\n\t"
    "v_mfma_f32_16x16x32_bf16 v[76:79], v[200:203], v[188:191], v[76:79]\n\t"
    "s_waitcnt lgkmcnt(4)\n\t"
    "v_mfma_f32_16x16x32_bf16 v[80:83], v[204:207], v[176:179], v[80:83]\n\t"
    "ds_read_b64_tr_b16 v[200:201], v212 offset:53248\n\t"
    "v_mfma_f32_16x16x32_bf16 v[84:87], v[204:207], v[180:183], v[84:87]\n\t"
    "ds_read_b64_tr_b16 v[202:203], v212 offset:53760\n\t"
    "v_mfma_f32_16x16x32_bf16 v[88:91], v[204:207], v[184:187], v[88:91]\n\t"
    "v_mfma_f32_16x16x32_bf16 v[92:95], v[204:207], v[188:191], v[92:95]\n\t"
    "s_waitcnt vmcnt(6)\n\t"
    "s_barrier\n\t"
    "s_waitcnt lgkmcnt(4)\n\t"
    "v_mfma_f32_16x16x32_bf16 v[96:99], v[192:195], v[176:179], v[96:99]\n\t"
    "ds_read_b64_tr_b16 v[204:205], v213 offset:53248\n\t"
    "v_mfma_f32_16x16x32_bf16 v[100:103], v[192:195], v[180:183], v[100:103]\n\t"
    "ds_read_b64_tr_b16 v[206:207], v213 offset:53760\n\t"
    "v_mfma_f32_16x16x32_bf16 v[104:107], v[192:195], v[184:187], v[104:107]\n\t"
    "ds_read_b128 v[160:163], v208 offset:0\n\t"
    "s_add_u32 m0, %[sd0], 73728\n\t"
    "v_mfma_f32_16x16x32_bf16 v[108:111], v[192:195], v[188:191], v[108:111]\n\t"
    "buffer_load_dwordx4 v214, %[r0], %[so0] offen sc1 lds\n\t"
    "s_add_u32 %[so0], %[so0], %[st0]\n\t"
    "s_waitcnt lgkmcnt(5)\n\t"
    "v_mfma_f32_16x16x32_bf16 v[112:115], v[196:199], v[176:179], v[112:115]\n\t"
    "ds_read_b64_tr_b16 v[192:193], v209 offset:0\n\t"
    "v_mfma_f32_16x16x32_bf16 v[116:119], v[196:199], v[180:183], v[116:119]\n\t"
    "ds_read_b64_tr_b16 v[194:195], v209 offset:512\n\t"
    "v_mfma_f32_16x16x32_bf16 v[120:123], v[196:199], v[184:187], v[120:123]\n\t"
    "ds_read_b128 v[164:167], v208 offset:1024\n\t"
    "s_add_u32 m0, %[sd1], 73728\n\t"
    "v_mfma_f32_16x16x32_bf16 v[124:127], v[196:199], v[188:191], v[124:127]\n\t"
    "buffer_load_dwordx4 v215, %[r1], %[so1] offen sc1 lds\n\t"
    "s_add_u32 %[so1], %[so1], %[st1]\n\t"
    "s_waitcnt lgkmcnt(6)\n\t"
    "v_mfma_f32_16x16x32_bf16 v[128:131], v[200:203], v[176:179], v[128:131]\n\t"
    "ds_read_b64_tr_b16 v[196:197], v210 offset:0\n\t"
    "v_mfma_f32_16x16x32_bf16 v[132:135], v[200:203], v[180:183], v[132:135]\n\t"
    "ds_read_b64_tr_b16 v[198:199], v210 offset:512\n\t"
    "v_mfma_f32_16x16x32_bf16 v[136:139], v[200:203], v[184:187], v[136:139]\n\t"
    "ds_read_b128 v[168:171], v208 offset:2048\n\t"
    "s_add_u32 m0, %[sd2], 73728\n\t"
    "v_mfma_f32_16x16x32_bf16 v[140:143], v[200:203], v[188:191], v[140:143]\n\t"
    "buffer_load_dwordx4 v216, %[r2], %[so2] offen sc1 lds\n\t"
    "s_add_u32 %[so2], %[so2], %[st2]\n\t"
    "s_waitcnt lgkmcnt(7)\n\t"
    "v_mfma_f32_16x16x32_bf16 v[144:147], v[204:207], v[176:179], v[144:147]\n\t"
    "ds_read_b64_tr_b16 v[200:201], v209 offset:4096\n\t"
    "v_mfma_f32_16x16x32_bf16 v[148:151], v[204:207], v[180:183], v[148:151]\n\t"
    "ds_read_b64_tr_b16 v[202:203], v209 offset:4608\n\t"
    "v_mfma_f32_16x16x32_bf16 v[152:155], v[204:207], v[184:187], v[152:155]\n\t"
    "ds_read_b128 v[172:175], v208 offset:3072\n\t"
    "s_add_u32 m0, %[sd3], 73728\n\t"
    "v_mfma_f32_16x16x32_bf16 v[156:159], v[204:207], v[188:191], v[156:159]\n\t"
    "buffer_load_dwordx4 v217, %[r3], %[so3] offen nt lds\n\t"
    "s_add_u32 %[so3], %[so3], %[st3]\n\t"
    "s_sub_u32 %[cnt], %[cnt], 1\n\t"
    "s_cmp_lg_u32 %[cnt], 0\n\t"
    "s_cbranch_scc1 2b\n\t"
    "s_waitcnt lgkmcnt(7)\n\t"
    "v_mfma_f32_16x16x32_bf16 v[0:3], v[192:195], v[160:163], v[0:3]\n\t"
    "ds_read_b64_tr_b16 v[204:205], v210 offset:4096\n\t"
    "s_waitcnt lgkmcnt(7)\n\t"
    "v_mfma_f32_16x16x32_bf16 v[4:7], v[192:195], v[164:167], v[4:7]\n\t"
    "ds_read_b64_tr_b16 v[206:207], v210 offset:4608\n\t"
    "s_waitcnt lgkmcnt(5)\n\t"
    "v_mfma_f32_16x16x32_bf16 v[8:11], v[192:195], v[168:171], v[8:11]\n\t"
    "s_add_u32 m0, %[sd4], 73728\n\t"
    "s_waitcnt lgkmcnt(2)\n\t"
    "v_mfma_f32_16x16x32_bf16 v[12:15], v[192:195], v[172:175], v[12:15]\n\t"
    "buffer_load_dwordx4 v218, %[r4], %[so4] offen nt lds\n\t"
    "s_add_u32 %[so4], %[so4], %[st4]\n\t"
    "v_mfma_f32_16x16x32_bf16 v[16:19], v[196:199], v[160:163], v[16:19]\n\t"
    "ds_read_b64_tr_b16 v[192:193], v209 offset:8192\n\t"
    "v_mfma_f32_16x16x32_bf16 v[20:23], v[196:199], v[164:167], v[20:23]\n\t"
    "ds_read_b64_tr_b16 v[194:195], v209 offset:8704\n\t"
    "v_mfma_f32_16x16x32_bf16 v[24:27], v[196:199], v[168:171], v[24:27]\n\t"
    "s_add_u32 m0, %[sd5], 73728\n\t"
    "v_mfma_f32_16x16x32_bf16 v[28:31], v[196:199], v[172:175], v[28:31]\n\t"
    "buffer_load_dwordx4 v219, %[r5], %[so5] offen nt lds\n\t"
    "s_add_u32 %[so5], %[so5], %[st5]\n\t"
    "v_mfma_f32_16x16x32_bf16 v[32:35], v[200:203], v[160:163], v[32:35]\n\t"
    "ds_read_b64_tr_b16 v[196:197], v210 offset:8192\n\t"
    "v_mfma_f32_16x16x32_bf16 v[36:39], v[200:203], v[164:167], v[36:39]\n\t"
    "ds_read_b64_tr_b16 v[198:199], v210 offset:8704\n\t"
    "v_mfma_f32_16x16x32_bf16 v[40:43], v[200:203], v[168:171], v[40:43]\n\t"
    "v_mfma_f32_16x16x32_bf16 v[44:47], v[200:203], v[172:175], v[44:47]\n\t"
    "s_waitcnt lgkmcnt(4)\n\t"
    "v_mfma_f32_16x16x32_bf16 v[48:51], v[204:207], v[160:163], v[48:51]\n\t"
    "ds_read_b64_tr_b16 v[200:201], v209 offset:12288\n\t"
    "v_mfma_f32_16x16x32_bf16 v[52:55], v[204:207], v[164:167], v[52:55]\n\t"
    "ds_read_b64_tr_b16 v[202:203], v209 offset:12800\n\t"
    "v_mfma_f32_16x16x32_bf16 v[56:59], v[204:207], v[168:171], v[56:59]\n\t"
    "v_mfma_f32_16x16x32_bf16 v[60:63], v[204:207], v[172:175], v[60:63]\n\t"
    "s_waitcnt lgkmcnt(4)\n\t"
    "v_mfma_f32_16x16x32_bf16 v[64:67], v[192:195], v[160:163], v[64:67]\n\t"
    "ds_read_b64_tr_b16 v[204:205], v210 offset:12288\n\t"
    "v_mfma_f32_16x16x32_bf16 v[68:71], v[192:195], v[164:167], v[68:71]\n\t"
    "ds_read_b64_tr_b16 v[206:207], v210 offset:12800\n\t"
    "v_mfma_f32_16x16x32_bf16 v[72:75], v[192:195], v[168:171], v[72:75]\n\t"
    "v_mfma_f32_16x16x32_bf16 v[76:79], v[192:195], v[172:175], v[76:79]\n\t"
    "s_waitcnt lgkmcnt(4)\n\t"
    "v_mfma_f32_16x16x32_bf16 v[80:83], v[196:199], v[160:163], v[80:83]\n\t"
    "ds_read_b64_tr_b16 v[192:193], v209 offset:16384\n\t"
    "v_mfma_f32_16x16x32_bf16 v[84:87], v[196:199], v[164:167], v[84:87]\n\t"
    "ds_read_b64_tr_b16 v[194:195], v209 offset:16896\n\t"
    "v_mfma_f32_16x16x32_bf16 v[88:91], v[196:199], v[168:171], v[88:91]\n\t"
    "v_mfma_f32_16x16x32_bf16 v[92:95], v[196:199], v[172:175], v[92:95]\n\t"
    "s_waitcnt vmcnt(6)\n\t"
    "s_barrier\n\t"
    "s_waitcnt lgkmcnt(4)\n\t"
    "v_mfma_f32_16x16x32_bf16 v[96:99], v[200:203], v[160:163], v[96:99]\n\t"
    "ds_read_b64_tr_b16 v[196:197], v210 offset:16384\n\t"
    "v_mfma_f32_16x16x32_bf16 v[100:103], v[200:203], v[164:167], v[100:103]\n\t"
    "ds_read_b64_tr_b16 v[198:199], v210 offset:16896\n\t"
    "v_mfma_f32_16x16x32_bf16 v[104:107], v[200:203], v[168:171], v[104:107]\n\t"
    "ds_read_b128 v[176:179], v208 offset:36864\n\t"
    "s_add_u32 m0, %[sd0], 110592\n\t"
    "v_mfma_f32_16x16x32_bf16 v[108:111], v[200:203], v[172:175], v[108:111]\n\t"
    "buffer_load_dwordx4 v214, %[r0], %[so0] offen sc1 lds\n\t"
    "s_add_u32 %[so0], %[so0], %[st0]\n\t"
    "s_waitcnt lgkmcnt(5)\n\t"
    "v_mfma_f32_16x16x32_bf16 v[112:115], v[204:207], v[160:163], v[112:115]\n\t"
    "ds_read_b64_tr_b16 v[200:201], v209 offset:36864\n\t"
    "v_mfma_f32_16x16x32_bf16 v[116:119], v[204:207], v[164:167], v[116:119]\n\t"
    "ds_read_b64_tr_b16 v[202:203], v209 offset:37376\n\t"
    "v_mfma_f32_16x16x32_bf16 v[120:123], v[204:207], v[168:171], v[120:123]\n\t"
    "ds_read_b128 v[180:183], v208 offset:37888\n\t"
    "s_add_u32 m0, %[sd1], 110592\n\t"
    "v_mfma_f32_16x16x32_bf16 v[124:127], v[204:207], v[172:175], v[124:127]\n\t"
    "buffer_load_dwordx4 v215, %[r1], %[so1] offen sc1 lds\n\t"
    "s_add_u32 %[so1], %[so1], %[st1]\n\t"
    "s_waitcnt lgkmcnt(6)\n\t"
    "v_mfma_f32_16x16x32_bf16 v[128:131], v[192:195], v[160:163], v[128:131]\n\t"
    "ds_read_b64_tr_b16 v[204:205], v210 offset:36864\n\t"
    "v_mfma_f32_16x16x32_bf16 v[132:135], v[192:195], v[164:167], v[132:135]\n\t"
    "ds_read_b64_tr_b16 v[206:207], v210 offset:37376\n\t"
    "v_mfma_f32_16x16x32_bf16 v[136:139], v[192:195], v[168:171], v[136:139]\n\t"
    "ds_read_b128 v[184:187], v208 offset:38912\n\t"
    "s_add_u32 m0, %[sd2], 110592\n\t"
    "v_mfma_f32_16x16x32_bf16 v[140:143], v[192:195], v[172:175], v[140:143]\n\t"
    "buffer_load_dwordx4 v216, %[r2], %[so2] offen sc1 lds\n\t"
    "s_add_u32 %[so2], %[so2], %[st2]\n\t"
    "s_waitcnt lgkmcnt(7)\n\t"
    "v_mfma_f32_16x16x32_bf16 v[144:147], v[196:199], v[160:163], v[144:147]\n\t"
    "ds_read_b64_tr_b16 v[192:193], v209 offset:40960\n\t"
    "v_mfma_f32_16x16x32_bf16 v[148:151], v[196:199], v[164:167], v[148:151]\n\t"
    "ds_read_b64_tr_b16 v[194:195], v209 offset:41472\n\t"
    "v_mfma_f32_16x16x32_bf16 v[152:155], v[196:199], v[168:171], v[152:155]\n\t"
    "ds_read_b128 v[188:191], v208 offset:39936\n\t"
    "s_add_u32 m0, %[sd3], 110592\n\t"
    "v_mfma_f32_16x16x32_bf16 v[156:159], v[196:199], v[172:175], v[156:159]\n\t"
    "buffer_load_dwordx4 v217, %[r3], %[so3] offen nt lds\n\t"
    "s_add_u32 %[so3], %[so3], %[st3]\n\t"
    "s_waitcnt lgkmcnt(7)\n\t"
    "v_mfma_f32_16x16x32_bf16 v[0:3], v[200:203], v[176:179], v[0:3]\n\t"
    "ds_read_b64_tr_b16 v[196:197], v210 offset:40960\n\t"
    "s_waitcnt lgkmcnt(7)\n\t"
    "v_mfma_f32_16x16x32_bf16 v[4:7], v[200:203], v[180:183], v[4:7]\n\t"
    "ds_read_b64_tr_b16 v[198:199], v210 offset:41472\n\t"
    "s_waitcnt lgkmcnt(5)\n\t"
    "v_mfma_f32_16x16x32_bf16 v[8:11], v[200:203], v[184:187], v[8:11]\n\t"
    "s_add_u32 m0, %[sd4], 110592\n\t"
    "s_waitcnt lgkmcnt(2)\n\t"
    "v_mfma_f32_16x16x32_bf16 v[12:15], v[200:203], v[188:191], v[12:15]\n\t"
    "buffer_load_dwordx4 v218, %[r4], %[so4] offen nt lds\n\t"
    "s_add_u32 %[so4], %[so4], %[st4]\n\t"
    "v_mfma_f32_16x16x32_bf16 v[16:19], v[204:207], v[176:179], v[16:19]\n\t"
    "ds_read_b64_tr_b16 v[200:201], v209 offset:45056\n\t"
    "v_mfma_f32_16x16x32_bf16 v[20:23], v[204:207], v[180:183], v[20:23]\n\t"
    "ds_read_b64_tr_b16 v[202:203], v209 offset:45568\n\t"
    "v_mfma_f32_16x16x32_bf16 v[24:27], v[204:207], v[184:187], v[24:27]\n\t"
    "s_add_u32 m0, %[sd5], 110592\n\t"
    "v_mfma_f32_16x16x32_bf16 v[28:31], v[204:207], v[188:191], v[28:31]\n\t"
    "buffer_load_dwordx4 v219, %[r5], %[so5] offen nt lds\n\t"
    "s_add_u32 %[so5], %[so5], %[st5]\n\t"
    "v_mfma_f32_16x16x32_bf16 v[32:35], v[192:195], v[176:179], v[32:35]\n\t"
    "ds_read_b64_tr_b16 v[204:205], v210 offset:45056\n\t"
    "v_mfma_f32_16x16x32_bf16 v[36:39], v[192:195], v[180:183], v[36:39]\n\t"
    "ds_read_b64_tr_b16 v[206:207], v210 offset:45568\n\t"
    "v_mfma_f32_16x16x32_bf16 v[40:43], v[192:195], v[184:187], v[40:43]\n\t"
    "v_mfma_f32_16x16x32_bf16 v[44:47], v[192:195], v[188:191], v[44:47]\n\t"
    "s_waitcnt lgkmcnt(4)\n\t"
    "v_mfma_f32_16x16x32_bf16 v[48:51], v[196:199], v[176:179], v[48:51]\n\t"
    "ds_read_b64_tr_b16 v[192:193], v209 offset:49152\n\t"
    "v_mfma_f32_16x16x32_bf16 v[52:55], v[196:199], v[180:183], v[52:55]\n\t"
    "ds_read_b64_tr_b16 v[194:195], v209 offset:49664\n\t"
    "v_mfma_f32_16x16x32_bf16 v[56:59], v[196:199], v[184:187], v[56:59]\n\t"
    "v_mfma_f32_16x16x32_bf16 v[60:63], v[196:199], v[188:191], v[60:63]\n\t"
    "s_waitcnt lgkmcnt(4)\n\t"
    "v_mfma_f32_16x16x32_bf16 v[64:67], v[200:203], v[176:179], v[64:67]\n\t"
    "ds_read_b64_tr_b16 v[196:197], v210 offset:49152\n\t"
    "v_mfma_f32_16x16x32_bf16 v[68:71], v[200:203], v[180:183], v[68:71]\n\t"
    "ds_read_b64_tr_b16 v[198:199], v210 offset:49664\n\t"
    "v_mfma_f32_16x16x32_bf16 v[72:75], v[200:203], v[184:187], v[72:75]\n\t"
    "v_mfma_f32_16x16x32_bf16 v[76:79], v[200:203], v[188:191], v[76:79]\n\t"
    "s_waitcnt lgkmcnt(4)\n\t"
    "v_mfma_f32_16x16x32_bf16 v[80:83], v[204:207], v[176:179], v[80:83]\n\t"
    "ds_read_b64_tr_b16 v[200:201], v209 offset:53248\n\t"
    "v_mfma_f32_16x16x32_bf16 v[84:87], v[204:207], v[180:183], v[84:87]\n\t"
    "ds_read_b64_tr_b16 v[202:203], v209 offset:53760\n\t"
    "v_mfma_f32_16x16x32_bf16 v[88:91], v[204:207], v[184:187], v[88:91]\n\t"
    "v_mfma_f32_16x16x32_bf16 v[92:95], v[204:207], v[188:191], v[92:95]\n\t"
    "s_waitcnt vmcnt(6)\n\t"
    "s_barrier\n\t"
    "s_waitcnt lgkmcnt(4)\n\t"
    "v_mfma_f32_16x16x32_bf16 v[96:99], v[192:195], v[176:179], v[96:99]\n\t"
    "ds_read_b64_tr_b16 v[204:205], v210 offset:53248\n\t"
    "v_mfma_f32_16x16x32_bf16 v[100:103], v[192:195], v[180:183], v[100:103]\n\t"
    "ds_read_b64_tr_b16 v[206:207], v210 offset:53760\n\t"
    "v_mfma_f32_16x16x32_bf16 v[104:107], v[192:195], v[184:187], v[104:107]\n\t"
    "ds_read_b128 v[160:163], v211 offset:0\n\t"
    "v_mfma_f32_16x16x32_bf16 v[108:111], v[192:195], v[188:191], v[108:111]\n\t"
    "s_waitcnt lgkmcnt(5)\n\t"
    "v_mfma_f32_16x16x32_bf16 v[112:115], v[196:199], v[176:179], v[112:115]\n\t"
    "ds_read_b64_tr_b16 v[192:193], v212 offset:0\n\t"
    "v_mfma_f32_16x16x32_bf16 v[116:119], v[196:199], v[180:183], v[116:119]\n\t"
    "ds_read_b64_tr_b16 v[194:195], v212 offset:512\n\t"
    "v_mfma_f32_16x16x32_bf16 v[120:123], v[196:199], v[184:187], v[120:123]\n\t"
    "ds_read_b128 v[164:167], v211 offset:1024\n\t"
    "v_mfma_f32_16x16x32_bf16 v[124:127], v[196:199], v[188:191], v[124:127]\n\t"
    "s_waitcnt lgkmcnt(6)\n\t"
    "v_mfma_f32_16x16x32_bf16 v[128:131], v[200:203], v[176:179], v[128:131]\n\t"
    "ds_read_b64_tr_b16 v[196:197], v213 offset:0\n\t"
    "v_mfma_f32_16x16x32_bf16 v[132:135], v[200:203], v[180:183], v[132:135]\n\t"
    "ds_read_b64_tr_b16 v[198:199], v213 offset:512\n\t"
    "v_mfma_f32_16x16x32_bf16 v[136:139], v[200:203], v[184:187], v[136:139]\n\t"
    "ds_read_b128 v[168:171], v211 offset:2048\n\t"
    "v_mfma_f32_16x16x32_bf16 v[140:143], v[200:203], v[188:191], v[140:143]\n\t"
    "s_waitcnt lgkmcnt(7)\n\t"
    "v_mfma_f32_16x16x32_bf16 v[144:147], v[204:207], v[176:179], v[144:147]\n\t"
    "ds_read_b64_tr_b16 v[200:201], v212 offset:4096\n\t"
    "v_mfma_f32_16x16x32_bf16 v[148:151], v[204:207], v[180:183], v[148:151]\n\t"
    "ds_read_b64_tr_b16 v[202:203], v212 offset:4608\n\t"
    "v_mfma_f32_16x16x32_bf16 v[152:155], v[204:207], v[184:187], v[152:155]\n\t"
    "ds_read_b128 v[172:175], v211 offset:3072\n\t"
    "v_mfma_f32_16x16x32_bf16 v[156:159], v[204:207], v[188:191], v[156:159]\n\t"
    "s_waitcnt lgkmcnt(7)\n\t"
    "v_mfma_f32_16x16x32_bf16 v[0:3], v[192:195], v[160:163], v[0:3]\n\t"
    "ds_read_b64_tr_b16 v[204:205], v213 offset:4096\n\t"
    "s_waitcnt lgkmcnt(7)\n\t"
    "v_mfma_f32_16x16x32_bf16 v[4:7], v[192:195], v[164:167], v[4:7]\n\t"
    "ds_read_b64_tr_b16 v[206:207], v213 offset:4608\n\t"
    "s_waitcnt lgkmcnt(5)\n\t"
    "v_mfma_f32_16x16x32_bf16 v[8:11], v[192:195], v[168:171], v[8:11]\n\t"
    "s_waitcnt lgkmcnt(2)\n\t"
    "v_mfma_f32_16x16x32_bf16 v[12:15], v[192:195], v[172:175], v[12:15]\n\t"
    "v_mfma_f32_16x16x32_bf16 v[16:19], v[196:199], v[160:163], v[16:19]\n\t"
    "ds_read_b64_tr_b16 v[192:193], v212 offset:8192\n\t"
    "v_mfma_f32_16x16x32_bf16 v[20:23], v[196:199], v[164:167], v[20:23]\n\t"
    "ds_read_b64_tr_b16 v[194:195], v212 offset:8704\n\t"
    "v_mfma_f32_16x16x32_bf16 v[24:27], v[196:199], v[168:171], v[24:27]\n\t"
    "v_mfma_f32_16x16x32_bf16 v[28:31], v[196:199], v[172:175], v[28:31]\n\t"
    "v_mfma_f32_16x16x32_bf16 v[32:35], v[200:203], v[160:163], v[32:35]\n\t"
    "ds_read_b64_tr_b16 v[196:197], v213 offset:8192\n\t"
    "v_mfma_f32_16x16x32_bf16 v[36:39], v[200:203], v[164:167], v[36:39]\n\t"
    "ds_read_b64_tr_b16 v[198:199], v213 offset:8704\n\t"
    "v_mfma_f32_16x16x32_bf16 v[40:43], v[200:203], v[168:171], v[40:43]\n\t"
    "v_mfma_f32_16x16x32_bf16 v[44:47], v[200:203], v[172:175], v[44:47]\n\t"
    "s_waitcnt lgkmcnt(4)\n\t"
    "v_mfma_f32_16x16x32_bf16 v[48:51], v[204:207], v[160:163], v[48:51]\n\t"
    "ds_read_b64_tr_b16 v[200:201], v212 offset:12288\n\t"
    "v_mfma_f32_16x16x32_bf16 v[52:55], v[204:207], v[164:167], v[52:55]\n\t"
    "ds_read_b64_tr_b16 v[202:203], v212 offset:12800\n\t"
    "v_mfma_f32_16x16x32_bf16 v[56:59], v[204:207], v[168:171], v[56:59]\n\t"
    "v_mfma_f32_16x16x32_bf16 v[60:63], v[204:207], v[172:175], v[60:63]\n\t"
    "s_waitcnt lgkmcnt(4)\n\t"
    "v_mfma_f32_16x16x32_bf16 v[64:67], v[192:195], v[160:163], v[64:67]\n\t"
    "ds_read_b64_tr_b16 v[204:205], v213 offset:12288\n\t"
    "v_mfma_f32_16x16x32_bf16 v[68:71], v[192:195], v[164:167], v[68:71]\n\t"
    "ds_read_b64_tr_b16 v[206:207], v213 offset:12800\n\t"
    "v_mfma_f32_16x16x32_bf16 v[72:75], v[192:195], v[168:171], v[72:75]\n\t"
    "v_mfma_f32_16x16x32_bf16 v[76:79], v[192:195], v[172:175], v[76:79]\n\t"
    "s_waitcnt lgkmcnt(4)\n\t"
    "v_mfma_f32_16x16x32_bf16 v[80:83], v[196:199], v[160:163], v[80:83]\n\t"
    "ds_read_b64_tr_b16 v[192:193], v212 offset:16384\n\t"
    "v_mfma_f32_16x16x32_bf16 v[84:87], v[196:199], v[164:167], v[84:87]\n\t"
    "ds_read_b64_tr_b16 v[194:195], v212 offset:16896\n\t"
    "v_mfma_f32_16x16x32_bf16 v[88:91], v[196:199], v[168:171], v[88:91]\n\t"
    "v_mfma_f32_16x16x32_bf16 v[92:95], v[196:199], v[172:175], v[92:95]\n\t"
    "s_waitcnt vmcnt(0)\n\t"
    "s_barrier\n\t"
    "s_waitcnt lgkmcnt(4)\n\t"
    "v_mfma_f32_16x16x32_bf16 v[96:99], v[200:203], v[160:163], v[96:99]\n\t"
    "ds_read_b64_tr_b16 v[196:197], v213 offset:16384\n\t"
    "v_mfma_f32_16x16x32_bf16 v[100:103], v[200:203], v[164:167], v[100:103]\n\t"
    "ds_read_b64_tr_b16 v[198:199], v213 offset:16896\n\t"
    "v_mfma_f32_16x16x32_bf16 v[104:107], v[200:203], v[168:171], v[104:107]\n\t"
    "ds_read_b128 v[176:179], v211 offset:36864\n\t"
    "v_mfma_f32_16x16x32_bf16 v[108:111], v[200:203], v[172:175], v[108:111]\n\t"
    "s_waitcnt lgkmcnt(5)\n\t"
    "v_mfma_f32_16x16x32_bf16 v[112:115], v[204:207], v[160:163], v[112:115]\n\t"
    "ds_read_b64_tr_b16 v[200:201], v212 offset:36864\n\t"
    "v_mfma_f32_16x16x32_bf16 v[116:119], v[204:207], v[164:167], v[116:119]\n\t"
    "ds_read_b64_tr_b16 v[202:203], v212 offset:37376\n\t"
    "v_mfma_f32_16x16x32_bf16 v[120:123], v[204:207], v[168:171], v[120:123]\n\t"
    "ds_read_b128 v[180:183], v211 offset:37888\n\t"
    "v_mfma_f32_16x16x32_bf16 v[124:127], v[204:207], v[172:175], v[124:127]\n\t"
    "s_waitcnt lgkmcnt(6)\n\t"
    "v_mfma_f32_16x16x32_bf16 v[128:131], v[192:195], v[160:163], v[128:131]\n\t"
    "ds_read_b64_tr_b16 v[204:205], v213 offset:36864\n\t"
    "v_mfma_f32_16x16x32_bf16 v[132:135], v[192:195], v[164:167], v[132:135]\n\t"
    "ds_read_b64_tr_b16 v[206:207], v213 offset:37376\n\t"
    "v_mfma_f32_16x16x32_bf16 v[136:139], v[192:195], v[168:171], v[136:139]\n\t"
    "ds_read_b128 v[184:187], v211 offset:38912\n\t"
    "v_mfma_f32_16x16x32_bf16 v[140:143], v[192:195], v[172:175], v[140:143]\n\t"
    "s_waitcnt lgkmcnt(7)\n\t"
    "v_mfma_f32_16x16x32_bf16 v[144:147], v[196:199], v[160:163], v[144:147]\n\t"
    "ds_read_b64_tr_b16 v[192:193], v212 offset:40960\n\t"
    "v_mfma_f32_16x16x32_bf16 v[148:151], v[196:199], v[164:167], v[148:151]\n\t"
    "ds_read_b64_tr_b16 v[194:195], v212 offset:41472\n\t"
    "v_mfma_f32_16x16x32_bf16 v[152:155], v[196:199], v[168:171], v[152:155]\n\t"
    "ds_read_b128 v[188:191], v211 offset:39936\n\t"
    "v_mfma_f32_16x16x32_bf16 v[156:159], v[196:199], v[172:175], v[156:159]\n\t"
    "s_waitcnt lgkmcnt(7)\n\t"
    "v_mfma_f32_16x16x32_bf16 v[0:3], v[200:203], v[176:179], v[0:3]\n\t"
    "ds_read_b64_tr_b16 v[196:197], v213 offset:40960\n\t"
    "s_waitcnt lgkmcnt(7)\n\t"
    "v_mfma_f32_16x16x32_bf16 v[4:7], v[200:203], v[180:183], v[4:7]\n\t"
    "ds_read_b64_tr_b16 v[198:199], v213 offset:41472\n\t"
    "s_waitcnt lgkmcnt(5)\n\t"
    "v_mfma_f32_16x16x32_bf16 v[8:11], v[200:203], v[184:187], v[8:11]\n\t"
    "s_waitcnt lgkmcnt(2)\n\t"
    "v_mfma_f32_16x16x32_bf16 v[12:15], v[200:203], v[188:191], v[12:15]\n\t"
    "v_mfma_f32_16x16x32_bf16 v[16:19], v[204:207], v[176:179], v[16:19]\n\t"
    "ds_read_b64_tr_b16 v[200:201], v212 offset:45056\n\t"
    "v_mfma_f32_16x16x32_bf16 v[20:23], v[204:207], v[180:183], v[20:23]\n\t"
    "ds_read_b64_tr_b16 v[202:203], v212 offset:45568\n\t"
    "v_mfma_f32_16x16x32_bf16 v[24:27], v[204:207], v[184:187], v[24:27]\n\t"
    "v_mfma_f32_16x16x32_bf16 v[28:31], v[204:207], v[188:191], v[28:31]\n\t"
    "v_mfma_f32_16x16x32_bf16 v[32:35], v[192:195], v[176:179], v[32:35]\n\t"
    "ds_read_b64_tr_b16 v[204:205], v213 offset:45056\n\t"
    "v_mfma_f32_16x16x32_bf16 v[36:39], v[192:195], v[180:183], v[36:39]\n\t"
    "ds_read_b64_tr_b16 v[206:207], v213 offset:45568\n\t"
    "v_mfma_f32_16x16x32_bf16 v[40:43], v[192:195], v[184:187], v[40:43]\n\t"
    "v_mfma_f32_16x16x32_bf16 v[44:47], v[192:195], v[188:191], v[44:47]\n\t"
    "s_waitcnt lgkmcnt(4)\n\t"
    "v_mfma_f32_16x16x32_bf16 v[48:51], v[196:199], v[176:179], v[48:51]\n\t"
    "ds_read_b64_tr_b16 v[192:193], v212 offset:49152\n\t"
    "v_mfma_f32_16x16x32_bf16 v[52:55], v[196:199], v[180:183], v[52:55]\n\t"
    "ds_read_b64_tr_b16 v[194:195], v212 offset:49664\n\t"
    "v_mfma_f32_16x16x32_bf16 v[56:59], v[196:199], v[184:187], v[56:59]\n\t"
    "v_mfma_f32_16x16x32_bf16 v[60:63], v[196:199], v[188:191], v[60:63]\n\t"
    "s_waitcnt lgkmcnt(4)\n\t"
    "v_mfma_f32_16x16x32_bf16 v[64:67], v[200:203], v[176:179], v[64:67]\n\t"
    "ds_read_b64_tr_b16 v[196:197], v213 offset:49152\n\t"
    "v_mfma_f32_16x16x32_bf16 v[68:71], v[200:203], v[180:183], v[68:71]\n\t"
    "ds_read_b64_tr_b16 v[198:199], v213 offset:49664\n\t"
    "v_mfma_f32_16x16x32_bf16 v[72:75], v[200:203], v[184:187], v[72:75]\n\t"
    "v_mfma_f32_16x16x32_bf16 v[76:79], v[200:203], v[188:191], v[76:79]\n\t"
    "s_waitcnt lgkmcnt(4)\n\t"
    "v_mfma_f32_16x16x32_bf16 v[80:83], v[204:207], v[176:179], v[80:83]\n\t"
    "ds_read_b64_tr_b16 v[200:201], v212 offset:53248\n\t"
    "v_mfma_f32_16x16x32_bf16 v[84:87], v[204:207], v[180:183], v[84:87]\n\t"
    "ds_read_b64_tr_b16 v[202:203], v212 offset:53760\n\t"
    "v_mfma_f32_16x16x32_bf16 v[88:91], v[204:207], v[184:187], v[88:91]\n\t"
    "v_mfma_f32_16x16x32_bf16 v[92:95], v[204:207], v[188:191], v[92:95]\n\t"
    "s_waitcnt lgkmcnt(4)\n\t"
    "v_mfma_f32_16x16x32_bf16 v[96:99], v[192:195], v[176:179], v[96:99]\n\t"
    "ds_read_b64_tr_b16 v[204:205], v213 offset:53248\n\t"
    "v_mfma_f32_16x16x32_bf16 v[100:103], v[192:195], v[180:183], v[100:103]\n\t"
    "ds_read_b64_tr_b16 v[206:207], v213 offset:53760\n\t"
    "v_mfma_f32_16x16x32_bf16 v[104:107], v[192:195], v[184:187], v[104:107]\n\t"
    "v_mfma_f32_16x16x32_bf16 v[108:111], v[192:195], v[188:191], v[108:111]\n\t"
    "s_waitcnt lgkmcnt(4)\n\t"
    "v_mfma_f32_16x16x32_bf16 v[112:115], v[196:199], v[176:179], v[112:115]\n\t"
    "v_mfma_f32_16x16x32_bf16 v[116:119], v[196:199], v[180:183], v[116:119]\n\t"
    "v_mfma_f32_16x16x32_bf16 v[120:123], v[196:199], v[184:187], v[120:123]\n\t"
    "v_mfma_f32_16x16x32_bf16 v[124:127], v[196:199], v[188:191], v[124:127]\n\t"
    "s_waitcnt lgkmcnt(2)\n\t"
    "v_mfma_f32_16x16x32_bf16 v[128:131], v[200:203], v[176:179], v[128:131]\n\t"
    "v_mfma_f32_16x16x32_bf16 v[132:135], v[200:203], v[180:183], v[132:135]\n\t"
    "v_mfma_f32_16x16x32_bf16 v[136:139], v[200:203], v[184:187], v[136:139]\n\t"
    "v_mfma_f32_16x16x32_bf16 v[140:143], v[200:203], v[188:191], v[140:143]\n\t"
    "s_waitcnt lgkmcnt(0)\n\t"
    "v_mfma_f32_16x16x32_bf16 v[144:147], v[204:207], v[176:179], v[144:147]\n\t"
    "v_mfma_f32_16x16x32_bf16 v[148:151], v[204:207], v[180:183], v[148:151]\n\t"
    "v_mfma_f32_16x16x32_bf16 v[152:155], v[204:207], v[184:187], v[152:155]\n\t"
    "v_mfma_f32_16x16x32_bf16 v[156:159], v[204:207], v[188:191], v[156:159]\n\t"
    "s_nop 7\n\t"
    "s_nop 7\n\t"
        : "={v[0:15]}"(acc0), "={v[16:31]}"(acc1), "={v[32:47]}"(acc2), "={v[48:63]}"(acc3), "={v[64:79]}"(acc4), "={v[80:95]}"(acc5), "={v[96:111]}"(acc6), "={v[112:127]}"(acc7), "={v[128:143]}"(acc8), "={v[144:159]}"(acc9), [so0] "+&s"(so[0]), [so1] "+&s"(so[1]), [so2] "+&s"(so[2]), [so3] "+&s"(so[3]), [so4] "+&s"(so[4]), [so5] "+&s"(so[5]), [cnt] "=&s"(cnt)
        : "{v208}"(vA[0]), "{v209}"(vC[0][0]), "{v210}"(vC[0][1]), "{v211}"(vA[1]), "{v212}"(vC[1][0]), "{v213}"(vC[1][1]), "{v214}"(vS[0]), "{v215}"(vS[1]), "{v216}"(vS[2]), "{v217}"(vS[3]), "{v218}"(vS[4]), "{v219}"(vS[5]), [r0] "s"(rs[0]), [st0] "s"(st[0]), [sd0] "s"(sd[0]), [r1] "s"(rs[1]), [st1] "s"(st[1]), [sd1] "s"(sd[1]), [r2] "s"(rs[2]), [st2] "s"(st[2]), [sd2] "s"(sd[2]), [r3] "s"(rs[3]), [st3] "s"(st[3]), [sd3] "s"(sd[3]), [r4] "s"(rs[4]), [st4] "s"(st[4]), [sd4] "s"(sd[4]), [r5] "s"(rs[5]), [st5] "s"(st[5]), [sd5] "s"(sd[5])
        : "memory", "scc", "v160", "v161", "v162", "v163", "v164", "v165", "v166", "v167", "v168", "v169", "v170", "v171", "v172", "v173", "v174", "v175", "v176", "v177", "v178", "v179", "v180", "v181", "v182", "v183", "v184", "v185", "v186", "v187", "v188", "v189", "v190", "v191", "v192", "v193", "v194", "v195", "v196", "v197", "v198", "v199", "v200", "v201", "v202", "v203", "v204", "v205", "v206", "v207");
    } else {
      unsigned cnt;

      const i32x4 rpd = make_srd(wd, F_ * D_ * 2), rpo = make_srd(wo, D_ * D_ * 2);
      unsigned vPf = lane < 16 ? (unsigned)(b * 32 + (w - 6) * 16 + lane) * 128u : 0x80000000u;
      const unsigned vPo = lane < 32 ? (unsigned)(b * 64 + (w - 6) * 32 + lane) * 128u : 0x80000000u;
      const i32x4 rcode = make_srd((const void*)(pc0 + lenA), lenB);
      const unsigned vCd0 = ((w - 6) * 128 + lane) * 128, vCd1 = vCd0 + 64 * 128;
      asm volatile(
    "s_barrier\n\t"
    "ds_read_b128 v[160:163], v208 offset:0\n\t"
    "ds_read_b128 v[164:167], v208 offset:1024\n\t"
    "ds_read_b128 v[168:171], v208 offset:2048\n\t"
    "ds_read_b128 v[172:175], v208 offset:3072\n\t"
    "ds_read_b64_tr_b16 v[192:193], v209 offset:0\n\t"
    "ds_read_b64_tr_b16 v[194:195], v209 offset:512\n\t"
    "ds_read_b64_tr_b16 v[196:197], v210 offset:0\n\t"
    "ds_read_b64_tr_b16 v[198:199], v210 offset:512\n\t"
    "ds_read_b64_tr_b16 v[200:201], v209 offset:4096\n\t"
    "ds_read_b64_tr_b16 v[202:203], v209 offset:4608\n\t"
    "s_waitcnt lgkmcnt(4)\n\t"
    "v_mfma_f32_16x16x32_bf16 v[0:3], v[192:195], v[160:163], 0\n\t"
    "ds_read_b64_tr_b16 v[204:205], v210 offset:4096\n\t"
    "v_mfma_f32_16x16x32_bf16 v[4:7], v[192:195], v[164:167], 0\n\t"
    "ds_read_b64_tr_b16 v[206:207], v210 offset:4608\n\t"
    "v_mfma_f32_16x16x32_bf16 v[8:11], v[192:195], v[168:171], 0\n\t"
    "v_mfma_f32_16x16x32_bf16 v[12:15], v[192:195], v[172:175], 0\n\t"
    "s_waitcnt lgkmcnt(4)\n\t"
    "v_mfma_f32_16x16x32_bf16 v[16:19], v[196:199], v[160:163], 0\n\t"
    "ds_read_b64_tr_b16 v[192:193], v209 offset:8192\n\t"
    "v_mfma_f32_16x16x32_bf16 v[20:23], v[196:199], v[164:167], 0\n\t"
    "ds_read_b64_tr_b16 v[194:195], v209 offset:8704\n\t"
    "v_mfma_f32_16x16x32_bf16 v[24:27], v[196:199], v[168:171], 0\n\t"
    "v_mfma_f32_16x16x32_bf16 v[28:31], v[196:199], v[172:175], 0\n\t"
    "buffer_load_dword v216, v214, %[rpd], 0 offen\n\t"
    "v_add_u32 v214, 1048576, v214\n\t"
    "s_waitcnt lgkmcnt(4)\n\t"
    "v_mfma_f32_16x16x32_bf16 v[32:35], v[200:203], v[160:163], 0\n\t"
    "ds_read_b64_tr_b16 v[196:197], v210 offset:8192\n\t"
    "v_mfma_f32_16x16x32_bf16 v[36:39], v[200:203], v[164:167], 0\n\t"
    "ds_read_b64_tr_b16 v[198:199], v210 offset:8704\n\t"
    "v_mfma_f32_16x16x32_bf16 v[40:43], v[200:203], v[168:171], 0\n\t"
    "v_mfma_f32_16x16x32_bf16 v[44:47], v[200:203], v[172:175], 0\n\t"
    "s_waitcnt lgkmcnt(4)\n\t"
    "v_mfma_f32_16x16x32_bf16 v[48:51], v[204:207], v[160:163], 0\n\t"
    "ds_read_b64_tr_b16 v[200:201], v209 offset:12288\n\t"
    "v_mfma_f32_16x16x32_bf16 v[52:55], v[204:207], v[164:167], 0\n\t"
    "ds_read_b64_tr_b16 v[202:203], v209 offset:12800\n\t"
    "v_mfma_f32_16x16x32_bf16 v[56:59], v[204:207], v[168:171], 0\n\t"
    "v_mfma_f32_16x16x32_bf16 v[60:63], v[204:207], v[172:175], 0\n\t"
    "s_waitcnt lgkmcnt(4)\n\t"
    "v_mfma_f32_16x16x32_bf16 v[64:67], v[192:195], v[160:163], 0\n\t"
    "ds_read_b64_tr_b16 v[204:205], v210 offset:12288\n\t"
    "v_mfma_f32_16x16x32_bf16 v[68:71], v[192:195], v[164:167], 0\n\t"
    "ds_read_b64_tr_b16 v[206:207], v210 offset:12800\n\t"
    "v_mfma_f32_16x16x32_bf16 v[72:75], v[192:195], v[168:171], 0\n\t"
    "v_mfma_f32_16x16x32_bf16 v[76:79], v[192:195], v[172:175], 0\n\t"
    "s_waitcnt lgkmcnt(4)\n\t"
    "v_mfma_f32_16x16x32_bf16 v[80:83], v[196:199], v[160:163], 0\n\t"
    "ds_read_b64_tr_b16 v[192:193], v209 offset:16384\n\t"
    "v_mfma_f32_16x16x32_bf16 v[84:87], v[196:199], v[164:167], 0\n\t"
    "ds_read_b64_tr_b16 v[194:195], v209 offset:16896\n\t"
    "v_mfma_f32_16x16x32_bf16 v[88:91], v[196:199], v[168:171], 0\n\t"
    "v_mfma_f32_16x16x32_bf16 v[92:95], v[196:199], v[172:175], 0\n\t"
    "s_barrier\n\t"
    "s_waitcnt lgkmcnt(4)\n\t"
    "v_mfma_f32_16x16x32_bf16 v[96:99], v[200:203], v[160:163], 0\n\t"
    "ds_read_b64_tr_b16 v[196:197], v210 offset:16384\n\t"
    "v_mfma_f32_16x16x32_bf16 v[100:103], v[200:203], v[164:167], 0\n\t"
    "ds_read_b64_tr_b16 v[198:199], v210 offset:16896\n\t"
    "v_mfma_f32_16x16x32_bf16 v[104:107], v[200:203], v[168:171], 0\n\t"
    "ds_read_b128 v[176:179], v208 offset:36864\n\t"
    "v_mfma_f32_16x16x32_bf16 v[108:111], v[200:203], v[172:175], 0\n\t"
    "s_waitcnt lgkmcnt(5)\n\t"
    "v_mfma_f32_16x16x32_bf16 v[112:115], v[204:207], v[160:163], 0\n\t"
    "ds_read_b64_tr_b16 v[200:201], v209 offset:36864\n\t"
    "v_mfma_f32_16x16x32_bf16 v[116:119], v[204:207], v[164:167], 0\n\t"
    "ds_read_b64_tr_b16 v[202:203], v209 offset:37376\n\t"
    "v_mfma_f32_16x16x32_bf16 v[120:123], v[204:207], v[168:171], 0\n\t"
    "ds_read_b128 v[180:183], v208 offset:37888\n\t"
    "v_mfma_f32_16x16x32_bf16 v[124:127], v[204:207], v[172:175], 0\n\t"
    "s_waitcnt lgkmcnt(6)\n\t"
    "v_mfma_f32_16x16x32_bf16 v[128:131], v[192:195], v[160:163], 0\n\t"
    "ds_read_b64_tr_b16 v[204:205], v210 offset:36864\n\t"
    "v_mfma_f32_16x16x32_bf16 v[132:135], v[192:195], v[164:167], 0\n\t"
    "ds_read_b64_tr_b16 v[206:207], v210 offset:37376\n\t"
    "v_mfma_f32_16x16x32_bf16 v[136:139], v[192:195], v[168:171], 0\n\t"
    "ds_read_b128 v[184:187], v208 offset:38912\n\t"
    "v_mfma_f32_16x16x32_bf16 v[140:143], v[192:195], v[172:175], 0\n\t"
    "s_waitcnt lgkmcnt(7)\n\t"
    "v_mfma_f32_16x16x32_bf16 v[144:147], v[196:199], v[160:163], 0\n\t"
    "ds_read_b64_tr_b16 v[192:193], v209 offset:40960\n\t"
    "v_mfma_f32_16x16x32_bf16 v[148:151], v[196:199], v[164:167], 0\n\t"
    "ds_read_b64_tr_b16 v[194:195], v209 offset:41472\n\t"
    "v_mfma_f32_16x16x32_bf16 v[152:155], v[196:199], v[168:171], 0\n\t"
    "ds_read_b128 v[188:191], v208 offset:39936\n\t"
    "v_mfma_f32_16x16x32_bf16 v[156:159], v[196:199], v[172:175], 0\n\t"
    "s_waitcnt lgkmcnt(7)\n\t"
    "v_mfma_f32_16x16x32_bf16 v[0:3], v[200:203], v[176:179], v[0:3]\n\t"
    "ds_read_b64_tr_b16 v[196:197], v210 offset:40960\n\t"
    "s_waitcnt lgkmcnt(7)\n\t"
    "v_mfma_f32_16x16x32_bf16 v[4:7], v[200:203], v[180:183], v[4:7]\n\t"
    "ds_read_b64_tr_b16 v[198:199], v210 offset:41472\n\t"
    "s_waitcnt lgkmcnt(5)\n\t"
    "v_mfma_f32_16x16x32_bf16 v[8:11], v[200:203], v[184:187], v[8:11]\n\t"
    "s_waitcnt lgkmcnt(2)\n\t"
    "v_mfma_f32_16x16x32_bf16 v[12:15], v[200:203], v[188:191], v[12:15]\n\t"
    "v_mfma_f32_16x16x32_bf16 v[16:19], v[204:207], v[176:179], v[16:19]\n\t"
    "ds_read_b64_tr_b16 v[200:201], v209 offset:45056\n\t"
    "v_mfma_f32_16x16x32_bf16 v[20:23], v[204:207], v[180:183], v[20:23]\n\t"
    "ds_read_b64_tr_b16 v[202:203], v209 offset:45568\n\t"
    "v_mfma_f32_16x16x32_bf16 v[24:27], v[204:207], v[184:187], v[24:27]\n\t"
    "v_mfma_f32_16x16x32_bf16 v[28:31], v[204:207], v[188:191], v[28:31]\n\t"
    "v_mfma_f32_16x16x32_bf16 v[32:35], v[192:195], v[176:179], v[32:35]\n\t"
    "ds_read_b64_tr_b16 v[204:205], v210 offset:45056\n\t"
    "v_mfma_f32_16x16x32_bf16 v[36:39], v[192:195], v[180:183], v[36:39]\n\t"
    "ds_read_b64_tr_b16 v[206:207], v210 offset:45568\n\t"
    "v_mfma_f32_16x16x32_bf16 v[40:43], v[192:195], v[184:187], v[40:43]\n\t"
    "v_mfma_f32_16x16x32_bf16 v[44:47], v[192:195], v[188:191], v[44:47]\n\t"
    "s_waitcnt lgkmcnt(4)\n\t"
    "v_mfma_f32_16x16x32_bf16 v[48:51], v[196:199], v[176:179], v[48:51]\n\t"
    "ds_read_b64_tr_b16 v[192:193], v209 offset:49152\n\t"
    "v_mfma_f32_16x16x32_bf16 v[52:55], v[196:199], v[180:183], v[52:55]\n\t"
    "ds_read_b64_tr_b16 v[194:195], v209 offset:49664\n\t"
    "v_mfma_f32_16x16x32_bf16 v[56:59], v[196:199], v[184:187], v[56:59]\n\t"
    "v_mfma_f32_16x16x32_bf16 v[60:63], v[196:199], v[188:191], v[60:63]\n\t"
    "s_waitcnt lgkmcnt(4)\n\t"
    "v_mfma_f32_16x16x32_bf16 v[64:67], v[200:203], v[176:179], v[64:67]\n\t"
    "ds_read_b64_tr_b16 v[196:197], v210 offset:49152\n\t"
    "v_mfma_f32_16x16x32_bf16 v[68:71], v[200:203], v[180:183], v[68:71]\n\t"
    "ds_read_b64_tr_b16 v[198:199], v210 offset:49664\n\t"
    "v_mfma_f32_16x16x32_bf16 v[72:75], v[200:203], v[184:187], v[72:75]\n\t"
    "v_mfma_f32_16x16x32_bf16 v[76:79], v[200:203], v[188:191], v[76:79]\n\t"
    "s_waitcnt lgkmcnt(4)\n\t"
    "v_mfma_f32_16x16x32_bf16 v[80:83], v[204:207], v[176:179], v[80:83]\n\t"
    "ds_read_b64_tr_b16 v[200:201], v209 offset:53248\n\t"
    "v_mfma_f32_16x16x32_bf16 v[84:87], v[204:207], v[180:183], v[84:87]\n\t"
    "ds_read_b64_tr_b16 v[202:203], v209 offset:53760\n\t"
    "v_mfma_f32_16x16x32_bf16 v[88:91], v[204:207], v[184:187], v[88:91]\n\t"
    "v_mfma_f32_16x16x32_bf16 v[92:95], v[204:207], v[188:191], v[92:95]\n\t"
    "s_barrier\n\t"
    "s_waitcnt lgkmcnt(4)\n\t"
    "v_mfma_f32_16x16x32_bf16 v[96:99], v[192:195], v[176:179], v[96:99]\n\t"
    "ds_read_b64_tr_b16 v[204:205], v210 offset:53248\n\t"
    "v_mfma_f32_16x16x32_bf16 v[100:103], v[192:195], v[180:183], v[100:103]\n\t"
    "ds_read_b64_tr_b16 v[206:207], v210 offset:53760\n\t"
    "v_mfma_f32_16x16x32_bf16 v[104:107], v[192:195], v[184:187], v[104:107]\n\t"
    "ds_read_b128 v[160:163], v211 offset:0\n\t"
    "v_mfma_f32_16x16x32_bf16 v[108:111], v[192:195], v[188:191], v[108:111]\n\t"
    "s_waitcnt lgkmcnt(5)\n\t"
    "v_mfma_f32_16x16x32_bf16 v[112:115], v[196:199], v[176:179], v[112:115]\n\t"
    "ds_read_b64_tr_b16 v[192:193], v212 offset:0\n\t"
    "v_mfma_f32_16x16x32_bf16 v[116:119], v[196:199], v[180:183], v[116:119]\n\t"
    "ds_read_b64_tr_b16 v[194:195], v212 offset:512\n\t"
    "v_mfma_f32_16x16x32_bf16 v[120:123], v[196:199], v[184:187], v[120:123]\n\t"
    "ds_read_b128 v[164:167], v211 offset:1024\n\t"
    "v_mfma_f32_16x16x32_bf16 v[124:127], v[196:199], v[188:191], v[124:127]\n\t"
    "s_waitcnt lgkmcnt(6)\n\t"
    "v_mfma_f32_16x16x32_bf16 v[128:131], v[200:203], v[176:179], v[128:131]\n\t"
    "ds_read_b64_tr_b16 v[196:197], v213 offset:0\n\t"
    "v_mfma_f32_16x16x32_bf16 v[132:135], v[200:203], v[180:183], v[132:135]\n\t"
    "ds_read_b64_tr_b16 v[198:199], v213 offset:512\n\t"
    "v_mfma_f32_16x16x32_bf16 v[136:139], v[200:203], v[184:187], v[136:139]\n\t"
    "ds_read_b128 v[168:171], v211 offset:2048\n\t"
    "v_mfma_f32_16x16x32_bf16 v[140:143], v[200:203], v[188:191], v[140:143]\n\t"
    "s_waitcnt lgkmcnt(7)\n\t"
    "v_mfma_f32_16x16x32_bf16 v[144:147], v[204:207], v[176:179], v[144:147]\n\t"
    "ds_read_b64_tr_b16 v[200:201], v212 offset:4096\n\t"
    "v_mfma_f32_16x16x32_bf16 v[148:151], v[204:207], v[180:183], v[148:151]\n\t"
    "ds_read_b64_tr_b16 v[202:203], v212 offset:4608\n\t"
    "v_mfma_f32_16x16x32_bf16 v[152:155], v[204:207], v[184:187], v[152:155]\n\t"
    "ds_read_b128 v[172:175], v211 offset:3072\n\t"
    "v_mfma_f32_16x16x32_bf16 v[156:159], v[204:207], v[188:191], v[156:159]\n\t"
    "s_waitcnt lgkmcnt(7)\n\t"
    "v_mfma_f32_16x16x32_bf16 v[0:3], v[192:195], v[160:163], v[0:3]\n\t"
    "ds_read_b64_tr_b16 v[204:205], v213 offset:4096\n\t"
    "s_waitcnt lgkmcnt(7)\n\t"
    "v_mfma_f32_16x16x32_bf16 v[4:7], v[192:195], v[164:167], v[4:7]\n\t"
    "ds_read_b64_tr_b16 v[206:207], v213 offset:4608\n\t"
    "s_waitcnt lgkmcnt(5)\n\t"
    "v_mfma_f32_16x16x32_bf16 v[8:11], v[192:195], v[168:171], v[8:11]\n\t"
    "s_waitcnt lgkmcnt(2)\n\t"
    "v_mfma_f32_16x16x32_bf16 v[12:15], v[192:195], v[172:175], v[12:15]\n\t"
    "v_mfma_f32_16x16x32_bf16 v[16:19], v[196:199], v[160:163], v[16:19]\n\t"
    "ds_read_b64_tr_b16 v[192:193], v212 offset:8192\n\t"
    "v_mfma_f32_16x16x32_bf16 v[20:23], v[196:199], v[164:167], v[20:23]\n\t"
    "ds_read_b64_tr_b16 v[194:195], v212 offset:8704\n\t"
    "v_mfma_f32_16x16x32_bf16 v[24:27], v[196:199], v[168:171], v[24:27]\n\t"
    "v_mfma_f32_16x16x32_bf16 v[28:31], v[196:199], v[172:175], v[28:31]\n\t"
    "buffer_load_dword v216, v215, %[rpo], 0 offen\n\t"
    "v_mfma_f32_16x16x32_bf16 v[32:35], v[200:203], v[160:163], v[32:35]\n\t"
    "ds_read_b64_tr_b16 v[196:197], v213 offset:8192\n\t"
    "v_mfma_f32_16x16x32_bf16 v[36:39], v[200:203], v[164:167], v[36:39]\n\t"
    "ds_read_b64_tr_b16 v[198:199], v213 offset:8704\n\t"
    "v_mfma_f32_16x16x32_bf16 v[40:43], v[200:203], v[168:171], v[40:43]\n\t"
    "v_mfma_f32_16x16x32_bf16 v[44:47], v[200:203], v[172:175], v[44:47]\n\t"
    "s_waitcnt lgkmcnt(4)\n\t"
    "v_mfma_f32_16x16x32_bf16 v[48:51], v[204:207], v[160:163], v[48:51]\n\t"
    "ds_read_b64_tr_b16 v[200:201], v212 offset:12288\n\t"
    "v_mfma_f32_16x16x32_bf16 v[52:55], v[204:207], v[164:167], v[52:55]\n\t"
    "ds_read_b64_tr_b16 v[202:203], v212 offset:12800\n\t"
    "v_mfma_f32_16x16x32_bf16 v[56:59], v[204:207], v[168:171], v[56:59]\n\t"
    "v_mfma_f32_16x16x32_bf16 v[60:63], v[204:207], v[172:175], v[60:63]\n\t"
    "s_waitcnt lgkmcnt(4)\n\t"
    "v_mfma_f32_16x16x32_bf16 v[64:67], v[192:195], v[160:163], v[64:67]\n\t"
    "ds_read_b64_tr_b16 v[204:205], v213 offset:12288\n\t"
    "v_mfma_f32_16x16x32_bf16 v[68:71], v[192:195], v[164:167], v[68:71]\n\t"
    "ds_read_b64_tr_b16 v[206:207], v213 offset:12800\n\t"
    "v_mfma_f32_16x16x32_bf16 v[72:75], v[192:195], v[168:171], v[72:75]\n\t"
    "v_mfma_f32_16x16x32_bf16 v[76:79], v[192:195], v[172:175], v[76:79]\n\t"
    "s_waitcnt lgkmcnt(4)\n\t"
    "v_mfma_f32_16x16x32_bf16 v[80:83], v[196:199], v[160:163], v[80:83]\n\t"
    "ds_read_b64_tr_b16 v[192:193], v212 offset:16384\n\t"
    "v_mfma_f32_16x16x32_bf16 v[84:87], v[196:199], v[164:167], v[84:87]\n\t"
    "ds_read_b64_tr_b16 v[194:195], v212 offset:16896\n\t"
    "v_mfma_f32_16x16x32_bf16 v[88:91], v[196:199], v[168:171], v[88:91]\n\t"
    "v_mfma_f32_16x16x32_bf16 v[92:95], v[196:199], v[172:175], v[92:95]\n\t"
    "s_barrier\n\t"
    "s_waitcnt lgkmcnt(4)\n\t"
    "v_mfma_f32_16x16x32_bf16 v[96:99], v[200:203], v[160:163], v[96:99]\n\t"
    "ds_read_b64_tr_b16 v[196:197], v213 offset:16384\n\t"
    "v_mfma_f32_16x16x32_bf16 v[100:103], v[200:203], v[164:167], v[100:103]\n\t"
    "ds_read_b64_tr_b16 v[198:199], v213 offset:16896\n\t"
    "v_mfma_f32_16x16x32_bf16 v[104:107], v[200:203], v[168:171], v[104:107]\n\t"
    "ds_read_b128 v[176:179], v211 offset:36864\n\t"
    "v_mfma_f32_16x16x32_bf16 v[108:111], v[200:203], v[172:175], v[108:111]\n\t"
    "s_waitcnt lgkmcnt(5)\n\t"
    "v_mfma_f32_16x16x32_bf16 v[112:115], v[204:207], v[160:163], v[112:115]\n\t"
    "ds_read_b64_tr_b16 v[200:201], v212 offset:36864\n\t"
    "v_mfma_f32_16x16x32_bf16 v[116:119], v[204:207], v[164:167], v[116:119]\n\t"
    "ds_read_b64_tr_b16 v[202:203], v212 offset:37376\n\t"
    "v_mfma_f32_16x16x32_bf16 v[120:123], v[204:207], v[168:171], v[120:123]\n\t"
    "ds_read_b128 v[180:183], v211 offset:37888\n\t"
    "v_mfma_f32_16x16x32_bf16 v[124:127], v[204:207], v[172:175], v[124:127]\n\t"
    "s_waitcnt lgkmcnt(6)\n\t"
    "v_mfma_f32_16x16x32_bf16 v[128:131], v[192:195], v[160:163], v[128:131]\n\t"
    "ds_read_b64_tr_b16 v[204:205], v213 offset:36864\n\t"
    "v_mfma_f32_16x16x32_bf16 v[132:135], v[192:195], v[164:167], v[132:135]\n\t"
    "ds_read_b64_tr_b16 v[206:207], v213 offset:37376\n\t"
    "v_mfma_f32_16x16x32_bf16 v[136:139], v[192:195], v[168:171], v[136:139]\n\t"
    "ds_read_b128 v[184:187], v211 offset:38912\n\t"
    "v_mfma_f32_16x16x32_bf16 v[140:143], v[192:195], v[172:175], v[140:143]\n\t"
    "s_waitcnt lgkmcnt(7)\n\t"
    "v_mfma_f32_16x16x32_bf16 v[144:147], v[196:199], v[160:163], v[144:147]\n\t"
    "ds_read_b64_tr_b16 v[192:193], v212 offset:40960\n\t"
    "v_mfma_f32_16x16x32_bf16 v[148:151], v[196:199], v[164:167], v[148:151]\n\t"
    "ds_read_b64_tr_b16 v[194:195], v212 offset:41472\n\t"
    "v_mfma_f32_16x16x32_bf16 v[152:155], v[196:199], v[168:171], v[152:155]\n\t"
    "ds_read_b128 v[188:191], v211 offset:39936\n\t"
    "v_mfma_f32_16x16x32_bf16 v[156:159], v[196:199], v[172:175], v[156:159]\n\t"
    "s_waitcnt lgkmcnt(7)\n\t"
    "v_mfma_f32_16x16x32_bf16 v[0:3], v[200:203], v[176:179], v[0:3]\n\t"
    "ds_read_b64_tr_b16 v[196:197], v213 offset:40960\n\t"
    "s_waitcnt lgkmcnt(7)\n\t"
    "v_mfma_f32_16x16x32_bf16 v[4:7], v[200:203], v[180:183], v[4:7]\n\t"
    "ds_read_b64_tr_b16 v[198:199], v213 offset:41472\n\t"
    "s_waitcnt lgkmcnt(5)\n\t"
    "v_mfma_f32_16x16x32_bf16 v[8:11], v[200:203], v[184:187], v[8:11]\n\t"
    "s_waitcnt lgkmcnt(2)\n\t"
    "v_mfma_f32_16x16x32_bf16 v[12:15], v[200:203], v[188:191], v[12:15]\n\t"
    "v_mfma_f32_16x16x32_bf16 v[16:19], v[204:207], v[176:179], v[16:19]\n\t"
    "ds_read_b64_tr_b16 v[200:201], v212 offset:45056\n\t"
    "v_mfma_f32_16x16x32_bf16 v[20:23], v[204:207], v[180:183], v[20:23]\n\t"
    "ds_read_b64_tr_b16 v[202:203], v212 offset:45568\n\t"
    "v_mfma_f32_16x16x32_bf16 v[24:27], v[204:207], v[184:187], v[24:27]\n\t"
    "v_mfma_f32_16x16x32_bf16 v[28:31], v[204:207], v[188:191], v[28:31]\n\t"
    "v_mfma_f32_16x16x32_bf16 v[32:35], v[192:195], v[176:179], v[32:35]\n\t"
    "ds_read_b64_tr_b16 v[204:205], v213 offset:45056\n\t"
    "v_mfma_f32_16x16x32_bf16 v[36:39], v[192:195], v[180:183], v[36:39]\n\t"
    "ds_read_b64_tr_b16 v[206:207], v213 offset:45568\n\t"
    "v_mfma_f32_16x16x32_bf16 v[40:43], v[192:195], v[184:187], v[40:43]\n\t"
    "v_mfma_f32_16x16x32_bf16 v[44:47], v[192:195], v[188:191], v[44:47]\n\t"
    "s_waitcnt lgkmcnt(4)\n\t"
    "v_mfma_f32_16x16x32_bf16 v[48:51], v[196:199], v[176:179], v[48:51]\n\t"
    "ds_read_b64_tr_b16 v[192:193], v212 offset:49152\n\t"
    "v_mfma_f32_16x16x32_bf16 v[52:55], v[196:199], v[180:183], v[52:55]\n\t"
    "ds_read_b64_tr_b16 v[194:195], v212 offset:49664\n\t"
    "v_mfma_f32_16x16x32_bf16 v[56:59], v[196:199], v[184:187], v[56:59]\n\t"
    "v_mfma_f32_16x16x32_bf16 v[60:63], v[196:199], v[188:191], v[60:63]\n\t"
    "s_waitcnt lgkmcnt(4)\n\t"
    "v_mfma_f32_16x16x32_bf16 v[64:67], v[200:203], v[176:179], v[64:67]\n\t"
    "ds_read_b64_tr_b16 v[196:197], v213 offset:49152\n\t"
    "v_mfma_f32_16x16x32_bf16 v[68:71], v[200:203], v[180:183], v[68:71]\n\t"
    "ds_read_b64_tr_b16 v[198:199], v213 offset:49664\n\t"
    "v_mfma_f32_16x16x32_bf16 v[72:75], v[200:203], v[184:187], v[72:75]\n\t"
    "v_mfma_f32_16x16x32_bf16 v[76:79], v[200:203], v[188:191], v[76:79]\n\t"
    "s_waitcnt lgkmcnt(4)\n\t"
    "v_mfma_f32_16x16x32_bf16 v[80:83], v[204:207], v[176:179], v[80:83]\n\t"
    "ds_read_b64_tr_b16 v[200:201], v212 offset:53248\n\t"
    "v_mfma_f32_16x16x32_bf16 v[84:87], v[204:207], v[180:183], v[84:87]\n\t"
    "ds_read_b64_tr_b16 v[202:203], v212 offset:53760\n\t"
    "v_mfma_f32_16x16x32_bf16 v[88:91], v[204:207], v[184:187], v[88:91]\n\t"
    "v_mfma_f32_16x16x32_bf16 v[92:95], v[204:207], v[188:191], v[92:95]\n\t"
    "s_barrier\n\t"
    "s_waitcnt lgkmcnt(4)\n\t"
    "v_mfma_f32_16x16x32_bf16 v[96:99], v[192:195], v[176:179], v[96:99]\n\t"
    "ds_read_b64_tr_b16 v[204:205], v213 offset:53248\n\t"
    "v_mfma_f32_16x16x32_bf16 v[100:103], v[192:195], v[180:183], v[100:103]\n\t"
    "ds_read_b64_tr_b16 v[206:207], v213 offset:53760\n\t"
    "v_mfma_f32_16x16x32_bf16 v[104:107], v[192:195], v[184:187], v[104:107]\n\t"
    "ds_read_b128 v[160:163], v208 offset:0\n\t"
    "v_mfma_f32_16x16x32_bf16 v[108:111], v[192:195], v[188:191], v[108:111]\n\t"
    "s_waitcnt lgkmcnt(5)\n\t"
    "v_mfma_f32_16x16x32_bf16 v[112:115], v[196:199], v[176:179], v[112:115]\n\t"
    "ds_read_b64_tr_b16 v[192:193], v209 offset:0\n\t"
    "v_mfma_f32_16x16x32_bf16 v[116:119], v[196:199], v[180:183], v[116:119]\n\t"
    "ds_read_b64_tr_b16 v[194:195], v209 offset:512\n\t"
    "v_mfma_f32_16x16x32_bf16 v[120:123], v[196:199], v[184:187], v[120:123]\n\t"
    "ds_read_b128 v[164:167], v208 offset:1024\n\t"
    "v_mfma_f32_16x16x32_bf16 v[124:127], v[196:199], v[188:191], v[124:127]\n\t"
    "s_waitcnt lgkmcnt(6)\n\t"
    "v_mfma_f32_16x16x32_bf16 v[128:131], v[200:203], v[176:179], v[128:131]\n\t"
    "ds_read_b64_tr_b16 v[196:197], v210 offset:0\n\t"
    "v_mfma_f32_16x16x32_bf16 v[132:135], v[200:203], v[180:183], v[132:135]\n\t"
    "ds_read_b64_tr_b16 v[198:199], v210 offset:512\n\t"
    "v_mfma_f32_16x16x32_bf16 v[136:139], v[200:203], v[184:187], v[136:139]\n\t"
    "ds_read_b128 v[168:171], v208 offset:2048\n\t"
    "v_mfma_f32_16x16x32_bf16 v[140:143], v[200:203], v[188:191], v[140:143]\n\t"
    "s_waitcnt lgkmcnt(7)\n\t"
    "v_mfma_f32_16x16x32_bf16 v[144:147], v[204:207], v[176:179], v[144:147]\n\t"
    "ds_read_b64_tr_b16 v[200:201], v209 offset:4096\n\t"
    "v_mfma_f32_16x16x32_bf16 v[148:151], v[204:207], v[180:183], v[148:151]\n\t"
    "ds_read_b64_tr_b16 v[202:203], v209 offset:4608\n\t"
    "v_mfma_f32_16x16x32_bf16 v[152:155], v[204:207], v[184:187], v[152:155]\n\t"
    "ds_read_b128 v[172:175], v208 offset:3072\n\t"
    "v_mfma_f32_16x16x32_bf16 v[156:159], v[204:207], v[188:191], v[156:159]\n\t"
    "s_mov_b32 %[cnt], 6\n\t"
    "2:\n\t"
    "s_waitcnt lgkmcnt(7)\n\t"
    "v_mfma_f32_16x16x32_bf16 v[0:3], v[192:195], v[160:163], v[0:3]\n\t"
    "ds_read_b64_tr_b16 v[204:205], v210 offset:4096\n\t"
    "s_waitcnt lgkmcnt(7)\n\t"
    "v_mfma_f32_16x16x32_bf16 v[4:7], v[192:195], v[164:167], v[4:7]\n\t"
    "ds_read_b64_tr_b16 v[206:207], v210 offset:4608\n\t"
    "s_waitcnt lgkmcnt(5)\n\t"
    "v_mfma_f32_16x16x32_bf16 v[8:11], v[192:195], v[168:171], v[8:11]\n\t"
    "s_waitcnt lgkmcnt(2)\n\t"
    "v_mfma_f32_16x16x32_bf16 v[12:15], v[192:195], v[172:175], v[12:15]\n\t"
    "v_mfma_f32_16x16x32_bf16 v[16:19], v[196:199], v[160:163], v[16:19]\n\t"
    "ds_read_b64_tr_b16 v[192:193], v209 offset:8192\n\t"
    "v_mfma_f32_16x16x32_bf16 v[20:23], v[196:199], v[164:167], v[20:23]\n\t"
    "ds_read_b64_tr_b16 v[194:195], v209 offset:8704\n\t"
    "v_mfma_f32_16x16x32_bf16 v[24:27], v[196:199], v[168:171], v[24:27]\n\t"
    "v_mfma_f32_16x16x32_bf16 v[28:31], v[196:199], v[172:175], v[28:31]\n\t"
    "buffer_load_dword v216, v214, %[rpd], 0 offen\n\t"
    "v_add_u32 v214, 1048576, v214\n\t"
    "v_mfma_f32_16x16x32_bf16 v[32:35], v[200:203], v[160:163], v[32:35]\n\t"
    "ds_read_b64_tr_b16 v[196:197], v210 offset:8192\n\t"
    "v_mfma_f32_16x16x32_bf16 v[36:39], v[200:203], v[164:167], v[36:39]\n\t"
    "ds_read_b64_tr_b16 v[198:199], v210 offset:8704\n\t"
    "v_mfma_f32_16x16x32_bf16 v[40:43], v[200:203], v[168:171], v[40:43]\n\t"
    "v_mfma_f32_16x16x32_bf16 v[44:47], v[200:203], v[172:175], v[44:47]\n\t"
    "s_waitcnt lgkmcnt(4)\n\t"
    "v_mfma_f32_16x16x32_bf16 v[48:51], v[204:207], v[160:163], v[48:51]\n\t"
    "ds_read_b64_tr_b16 v[200:201], v209 offset:12288\n\t"
    "v_mfma_f32_16x16x32_bf16 v[52:55], v[204:207], v[164:167], v[52:55]\n\t"
    "ds_read_b64_tr_b16 v[202:203], v209 offset:12800\n\t"
    "v_mfma_f32_16x16x32_bf16 v[56:59], v[204:207], v[168:171], v[56:59]\n\t"
    "v_mfma_f32_16x16x32_bf16 v[60:63], v[204:207], v[172:175], v[60:63]\n\t"
    "s_waitcnt lgkmcnt(4)\n\t"
    "v_mfma_f32_16x16x32_bf16 v[64:67], v[192:195], v[160:163], v[64:67]\n\t"
    "ds_read_b64_tr_b16 v[204:205], v210 offset:12288\n\t"
    "v_mfma_f32_16x16x32_bf16 v[68:71], v[192:195], v[164:167], v[68:71]\n\t"
    "ds_read_b64_tr_b16 v[206:207], v210 offset:12800\n\t"
    "v_mfma_f32_16x16x32_bf16 v[72:75], v[192:195], v[168:171], v[72:75]\n\t"
    "v_mfma_f32_16x16x32_bf16 v[76:79], v[192:195], v[172:175], v[76:79]\n\t"
    "s_waitcnt lgkmcnt(4)\n\t"
    "v_mfma_f32_16x16x32_bf16 v[80:83], v[196:199], v[160:163], v[80:83]\n\t"
    "ds_read_b64_tr_b16 v[192:193], v209 offset:16384\n\t"
    "v_mfma_f32_16x16x32_bf16 v[84:87], v[196:199], v[164:167], v[84:87]\n\t"
    "ds_read_b64_tr_b16 v[194:195], v209 offset:16896\n\t"
    "v_mfma_f32_16x16x32_bf16 v[88:91], v[196:199], v[168:171], v[88:91]\n\t"
    "v_mfma_f32_16x16x32_bf16 v[92:95], v[196:199], v[172:175], v[92:95]\n\t"
    "s_barrier\n\t"
    "s_waitcnt lgkmcnt(4)\n\t"
    "v_mfma_f32_16x16x32_bf16 v[96:99], v[200:203], v[160:163], v[96:99]\n\t"
    "ds_read_b64_tr_b16 v[196:197], v210 offset:16384\n\t"
    "v_mfma_f32_16x16x32_bf16 v[100:103], v[200:203], v[164:167], v[100:103]\n\t"
    "ds_read_b64_tr_b16 v[198:199], v210 offset:16896\n\t"
    "v_mfma_f32_16x16x32_bf16 v[104:107], v[200:203], v[168:171], v[104:107]\n\t"
    "ds_read_b128 v[176:179], v208 offset:36864\n\t"
    "v_mfma_f32_16x16x32_bf16 v[108:111], v[200:203], v[172:175], v[108:111]\n\t"
    "s_waitcnt lgkmcnt(5)\n\t"
    "v_mfma_f32_16x16x32_bf16 v[112:115], v[204:207], v[160:163], v[112:115]\n\t"
    "ds_read_b64_tr_b16 v[200:201], v209 offset:36864\n\t"
    "v_mfma_f32_16x16x32_bf16 v[116:119], v[204:207], v[164:167], v[116:119]\n\t"
    "ds_read_b64_tr_b16 v[202:203], v209 offset:37376\n\t"
    "v_mfma_f32_16x16x32_bf16 v[120:123], v[204:207], v[168:171], v[120:123]\n\t"
    "ds_read_b128 v[180:183], v208 offset:37888\n\t"
    "v_mfma_f32_16x16x32_bf16 v[124:127], v[204:207], v[172:175], v[124:127]\n\t"
    "s_waitcnt lgkmcnt(6)\n\t"
    "v_mfma_f32_16x16x32_bf16 v[128:131], v[192:195], v[160:163], v[128:131]\n\t"
    "ds_read_b64_tr_b16 v[204:205], v210 offset:36864\n\t"
    "v_mfma_f32_16x16x32_bf16 v[132:135], v[192:195], v[164:167], v[132:135]\n\t"
    "ds_read_b64_tr_b16 v[206:207], v210 offset:37376\n\t"
    "v_mfma_f32_16x16x32_bf16 v[136:139], v[192:195], v[168:171], v[136:139]\n\t"
    "ds_read_b128 v[184:187], v208 offset:38912\n\t"
    "v_mfma_f32_16x16x32_bf16 v[140:143], v[192:195], v[172:175], v[140:143]\n\t"
    "s_waitcnt lgkmcnt(7)\n\t"
    "v_mfma_f32_16x16x32_bf16 v[144:147], v[196:199], v[160:163], v[144:147]\n\t"
    "ds_read_b64_tr_b16 v[192:193], v209 offset:40960\n\t"
    "v_mfma_f32_16x16x32_bf16 v[148:151], v[196:199], v[164:167], v[148:151]\n\t"
    "ds_read_b64_tr_b16 v[194:195], v209 offset:41472\n\t"
    "v_mfma_f32_16x16x32_bf16 v[152:155], v[196:199], v[168:171], v[152:155]\n\t"
    "ds_read_b128 v[188:191], v208 offset:39936\n\t"
    "v_mfma_f32_16x16x32_bf16 v[156:159], v[196:199], v[172:175], v[156:159]\n\t"
    "s_waitcnt lgkmcnt(7)\n\t"
    "v_mfma_f32_16x16x32_bf16 v[0:3], v[200:203], v[176:179], v[0:3]\n\t"
    "ds_read_b64_tr_b16 v[196:197], v210 offset:40960\n\t"
    "s_waitcnt lgkmcnt(7)\n\t"
    "v_mfma_f32_16x16x32_bf16 v[4:7], v[200:203], v[180:183], v[4:7]\n\t"
    "ds_read_b64_tr_b16 v[198:199], v210 offset:41472\n\t"
    "s_waitcnt lgkmcnt(5)\n\t"
    "v_mfma_f32_16x16x32_bf16 v[8:11], v[200:203], v[184:187], v[8:11]\n\t"
    "s_waitcnt lgkmcnt(2)\n\t"
    "v_mfma_f32_16x16x32_bf16 v[12:15], v[200:203], v[188:191], v[12:15]\n\t"
    "v_mfma_f32_16x16x32_bf16 v[16:19], v[204:207], v[176:179], v[16:19]\n\t"
    "ds_read_b64_tr_b16 v[200:201], v209 offset:45056\n\t"
    "v_mfma_f32_16x16x32_bf16 v[20:23], v[204:207], v[180:183], v[20:23]\n\t"
    "ds_read_b64_tr_b16 v[202:203], v209 offset:45568\n\t"
    "v_mfma_f32_16x16x32_bf16 v[24:27], v[204:207], v[184:187], v[24:27]\n\t"
    "v_mfma_f32_16x16x32_bf16 v[28:31], v[204:207], v[188:191], v[28:31]\n\t"
    "v_mfma_f32_16x16x32_bf16 v[32:35], v[192:195], v[176:179], v[32:35]\n\t"
    "ds_read_b64_tr_b16 v[204:205], v210 offset:45056\n\t"
    "v_mfma_f32_16x16x32_bf16 v[36:39], v[192:195], v[180:183], v[36:39]\n\t"
    "ds_read_b64_tr_b16 v[206:207], v210 offset:45568\n\t"
    "v_mfma_f32_16x16x32_bf16 v[40:43], v[192:195], v[184:187], v[40:43]\n\t"
    "v_mfma_f32_16x16x32_bf16 v[44:47], v[192:195], v[188:191], v[44:47]\n\t"
    "s_waitcnt lgkmcnt(4)\n\t"
    "v_mfma_f32_16x16x32_bf16 v[48:51], v[196:199], v[176:179], v[48:51]\n\t"
    "ds_read_b64_tr_b16 v[192:193], v209 offset:49152\n\t"
    "v_mfma_f32_16x16x32_bf16 v[52:55], v[196:199], v[180:183], v[52:55]\n\t"
    "ds_read_b64_tr_b16 v[194:195], v209 offset:49664\n\t"
    "v_mfma_f32_16x16x32_bf16 v[56:59], v[196:199], v[184:187], v[56:59]\n\t"
    "v_mfma_f32_16x16x32_bf16 v[60:63], v[196:199], v[188:191], v[60:63]\n\t"
    "s_waitcnt lgkmcnt(4)\n\t"
    "v_mfma_f32_16x16x32_bf16 v[64:67], v[200:203], v[176:179], v[64:67]\n\t"
    "ds_read_b64_tr_b16 v[196:197], v210 offset:49152\n\t"
    "v_mfma_f32_16x16x32_bf16 v[68:71], v[200:203], v[180:183], v[68:71]\n\t"
    "ds_read_b64_tr_b16 v[198:199], v210 offset:49664\n\t"
    "v_mfma_f32_16x16x32_bf16 v[72:75], v[200:203], v[184:187], v[72:75]\n\t"
    "v_mfma_f32_16x16x32_bf16 v[76:79], v[200:203], v[188:191], v[76:79]\n\t"
    "s_waitcnt lgkmcnt(4)\n\t"
    "v_mfma_f32_16x16x32_bf16 v[80:83], v[204:207], v[176:179], v[80:83]\n\t"
    "ds_read_b64_tr_b16 v[200:201], v209 offset:53248\n\t"
    "v_mfma_f32_16x16x32_bf16 v[84:87], v[204:207], v[180:183], v[84:87]\n\t"
    "ds_read_b64_tr_b16 v[202:203], v209 offset:53760\n\t"
    "v_mfma_f32_16x16x32_bf16 v[88:91], v[204:207], v[184:187], v[88:91]\n\t"
    "v_mfma_f32_16x16x32_bf16 v[92:95], v[204:207], v[188:191], v[92:95]\n\t"
    "s_barrier\n\t"
    "s_waitcnt lgkmcnt(4)\n\t"
    "v_mfma_f32_16x16x32_bf16 v[96:99], v[192:195], v[176:179], v[96:99]\n\t"
    "ds_read_b64_tr_b16 v[204:205], v210 offset:53248\n\t"
    "v_mfma_f32_16x16x32_bf16 v[100:103], v[192:195], v[180:183], v[100:103]\n\t"
    "ds_read_b64_tr_b16 v[206:207], v210 offset:53760\n\t"
    "v_mfma_f32_16x16x32_bf16 v[104:107], v[192:195], v[184:187], v[104:107]\n\t"
    "ds_read_b128 v[160:163], v211 offset:0\n\t"
    "v_mfma_f32_16x16x32_bf16 v[108:111], v[192:195], v[188:191], v[108:111]\n\t"
    "s_waitcnt lgkmcnt(5)\n\t"
    "v_mfma_f32_16x16x32_bf16 v[112:115], v[196:199], v[176:179], v[112:115]\n\t"
    "ds_read_b64_tr_b16 v[192:193], v212 offset:0\n\t"
    "v_mfma_f32_16x16x32_bf16 v[116:119], v[196:199], v[180:183], v[116:119]\n\t"
    "ds_read_b64_tr_b16 v[194:195], v212 offset:512\n\t"
    "v_mfma_f32_16x16x32_bf16 v[120:123], v[196:199], v[184:187], v[120:123]\n\t"
    "ds_read_b128 v[164:167], v211 offset:1024\n\t"
    "v_mfma_f32_16x16x32_bf16 v[124:127], v[196:199], v[188:191], v[124:127]\n\t"
    "s_waitcnt lgkmcnt(6)\n\t"
    "v_mfma_f32_16x16x32_bf16 v[128:131], v[200:203], v[176:179], v[128:131]\n\t"
    "ds_read_b64_tr_b16 v[196:197], v213 offset:0\n\t"
    "v_mfma_f32_16x16x32_bf16 v[132:135], v[200:203], v[180:183], v[132:135]\n\t"
    "ds_read_b64_tr_b16 v[198:199], v213 offset:512\n\t"
    "v_mfma_f32_16x16x32_bf16 v[136:139], v[200:203], v[184:187], v[136:139]\n\t"
    "ds_read_b128 v[168:171], v211 offset:2048\n\t"
    "v_mfma_f32_16x16x32_bf16 v[140:143], v[200:203], v[188:191], v[140:143]\n\t"
    "s_waitcnt lgkmcnt(7)\n\t"
    "v_mfma_f32_16x16x32_bf16 v[144:147], v[204:207], v[176:179], v[144:147]\n\t"
    "ds_read_b64_tr_b16 v[200:201], v212 offset:4096\n\t"
    "v_mfma_f32_16x16x32_bf16 v[148:151], v[204:207], v[180:183], v[148:151]\n\t"
    "ds_read_b64_tr_b16 v[202:203], v212 offset:4608\n\t"
    "v_mfma_f32_16x16x32_bf16 v[152:155], v[204:207], v[184:187], v[152:155]\n\t"
    "ds_read_b128 v[172:175], v211 offset:3072\n\t"
    "v_mfma_f32_16x16x32_bf16 v[156:159], v[204:207], v[188:191], v[156:159]\n\t"
    "s_waitcnt lgkmcnt(7)\n\t"
    "v_mfma_f32_16x16x32_bf16 v[0:3], v[192:195], v[160:163], v[0:3]\n\t"
    "ds_read_b64_tr_b16 v[204:205], v213 offset:4096\n\t"
    "s_waitcnt lgkmcnt(7)\n\t"
    "v_mfma_f32_16x16x32_bf16 v[4:7], v[192:195], v[164:167], v[4:7]\n\t"
    "ds_read_b64_tr_b16 v[206:207], v213 offset:4608\n\t"
    "s_waitcnt lgkmcnt(5)\n\t"
    "v_mfma_f32_16x16x32_bf16 v[8:11], v[192:195], v[168:171], v[8:11]\n\t"
    "s_waitcnt lgkmcnt(2)\n\t"
    "v_mfma_f32_16x16x32_bf16 v[12:15], v[192:195], v[172:175], v[12:15]\n\t"
    "v_mfma_f32_16x16x32_bf16 v[16:19], v[196:199], v[160:163], v[16:19]\n\t"
    "ds_read_b64_tr_b16 v[192:193], v212 offset:8192\n\t"
    "v_mfma_f32_16x16x32_bf16 v[20:23], v[196:199], v[164:167], v[20:23]\n\t"
    "ds_read_b64_tr_b16 v[194:195], v212 offset:8704\n\t"
    "v_mfma_f32_16x16x32_bf16 v[24:27], v[196:199], v[168:171], v[24:27]\n\t"
    "v_mfma_f32_16x16x32_bf16 v[28:31], v[196:199], v[172:175], v[28:31]\n\t"
    "v_mfma_f32_16x16x32_bf16 v[32:35], v[200:203], v[160:163], v[32:35]\n\t"
    "ds_read_b64_tr_b16 v[196:197], v213 offset:8192\n\t"
    "v_mfma_f32_16x16x32_bf16 v[36:39], v[200:203], v[164:167], v[36:39]\n\t"
    "ds_read_b64_tr_b16 v[198:199], v213 offset:8704\n\t"
    "v_mfma_f32_16x16x32_bf16 v[40:43], v[200:203], v[168:171], v[40:43]\n\t"
    "v_mfma_f32_16x16x32_bf16 v[44:47], v[200:203], v[172:175], v[44:47]\n\t"
    "s_waitcnt lgkmcnt(4)\n\t"
    "v_mfma_f32_16x16x32_bf16 v[48:51], v[204:207], v[160:163], v[48:51]\n\t"
    "ds_read_b64_tr_b16 v[200:201], v212 offset:12288\n\t"
    "v_mfma_f32_16x16x32_bf16 v[52:55], v[204:207], v[164:167], v[52:55]\n\t"
    "ds_read_b64_tr_b16 v[202:203], v212 offset:12800\n\t"
    "v_mfma_f32_16x16x32_bf16 v[56:59], v[204:207], v[168:171], v[56:59]\n\t"
    "v_mfma_f32_16x16x32_bf16 v[60:63], v[204:207], v[172:175], v[60:63]\n\t"
    "s_waitcnt lgkmcnt(4)\n\t"
    "v_mfma_f32_16x16x32_bf16 v[64:67], v[192:195], v[160:163], v[64:67]\n\t"
    "ds_read_b64_tr_b16 v[204:205], v213 offset:12288\n\t"
    "v_mfma_f32_16x16x32_bf16 v[68:71], v[192:195], v[164:167], v[68:71]\n\t"
    "ds_read_b64_tr_b16 v[206:207], v213 offset:12800\n\t"
    "v_mfma_f32_16x16x32_bf16 v[72:75], v[192:195], v[168:171], v[72:75]\n\t"
    "v_mfma_f32_16x16x32_bf16 v[76:79], v[192:195], v[172:175], v[76:79]\n\t"
    "s_waitcnt lgkmcnt(4)\n\t"
    "v_mfma_f32_16x16x32_bf16 v[80:83], v[196:199], v[160:163], v[80:83]\n\t"
    "ds_read_b64_tr_b16 v[192:193], v212 offset:16384\n\t"
    "v_mfma_f32_16x16x32_bf16 v[84:87], v[196:199], v[164:167], v[84:87]\n\t"
    "ds_read_b64_tr_b16 v[194:195], v212 offset:16896\n\t"
    "v_mfma_f32_16x16x32_bf16 v[88:91], v[196:199], v[168:171], v[88:91]\n\t"
    "v_mfma_f32_16x16x32_bf16 v[92:95], v[196:199], v[172:175], v[92:95]\n\t"
    "s_barrier\n\t"
    "s_waitcnt lgkmcnt(4)\n\t"
    "v_mfma_f32_16x16x32_bf16 v[96:99], v[200:203], v[160:163], v[96:99]\n\t"
    "ds_read_b64_tr_b16 v[196:197], v213 offset:16384\n\t"
    "v_mfma_f32_16x16x32_bf16 v[100:103], v[200:203], v[164:167], v[100:103]\n\t"
    "ds_read_b64_tr_b16 v[198:199], v213 offset:16896\n\t"
    "v_mfma_f32_16x16x32_bf16 v[104:107], v[200:203], v[168:171], v[104:107]\n\t"
    "ds_read_b128 v[176:179], v211 offset:36864\n\t"
    "v_mfma_f32_16x16x32_bf16 v[108:111], v[200:203], v[172:175], v[108:111]\n\t"
    "s_waitcnt lgkmcnt(5)\n\t"
    "v_mfma_f32_16x16x32_bf16 v[112:115], v[204:207], v[160:163], v[112:115]\n\t"
    "ds_read_b64_tr_b16 v[200:201], v212 offset:36864\n\t"
    "v_mfma_f32_16x16x32_bf16 v[116:119], v[204:207], v[164:167], v[116:119]\n\t"
    "ds_read_b64_tr_b16 v[202:203], v212 offset:37376\n\t"
    "v_mfma_f32_16x16x32_bf16 v[120:123], v[204:207], v[168:171], v[120:123]\n\t"
    "ds_read_b128 v[180:183], v211 offset:37888\n\t"
    "v_mfma_f32_16x16x32_bf16 v[124:127], v[204:207], v[172:175], v[124:127]\n\t"
    "s_waitcnt lgkmcnt(6)\n\t"
    "v_mfma_f32_16x16x32_bf16 v[128:131], v[192:195], v[160:163], v[128:131]\n\t"
    "ds_read_b64_tr_b16 v[204:205], v213 offset:36864\n\t"
    "v_mfma_f32_16x16x32_bf16 v[132:135], v[192:195], v[164:167], v[132:135]\n\t"
    "ds_read_b64_tr_b16 v[206:207], v213 offset:37376\n\t"
    "v_mfma_f32_16x16x32_bf16 v[136:139], v[192:195], v[168:171], v[136:139]\n\t"
    "ds_read_b128 v[184:187], v211 offset:38912\n\t"
    "v_mfma_f32_16x16x32_bf16 v[140:143], v[192:195], v[172:175], v[140:143]\n\t"
    "s_waitcnt lgkmcnt(7)\n\t"
    "v_mfma_f32_16x16x32_bf16 v[144:147], v[196:199], v[160:163], v[144:147]\n\t"
    "ds_read_b64_tr_b16 v[192:193], v212 offset:40960\n\t"
    "v_mfma_f32_16x16x32_bf16 v[148:151], v[196:199], v[164:167], v[148:151]\n\t"
    "ds_read_b64_tr_b16 v[194:195], v212 offset:41472\n\t"
    "v_mfma_f32_16x16x32_bf16 v[152:155], v[196:199], v[168:171], v[152:155]\n\t"
    "ds_read_b128 v[188:191], v211 offset:39936\n\t"
    "v_mfma_f32_16x16x32_bf16 v[156:159], v[196:199], v[172:175], v[156:159]\n\t"
    "s_waitcnt lgkmcnt(7)\n\t"
    "v_mfma_f32_16x16x32_bf16 v[0:3], v[200:203], v[176:179], v[0:3]\n\t"
    "ds_read_b64_tr_b16 v[196:197], v213 offset:40960\n\t"
    "s_waitcnt lgkmcnt(7)\n\t"
    "v_mfma_f32_16x16x32_bf16 v[4:7], v[200:203], v[180:183], v[4:7]\n\t"
    "ds_read_b64_tr_b16 v[198:199], v213 offset:41472\n\t"
    "s_waitcnt lgkmcnt(5)\n\t"
    "v_mfma_f32_16x16x32_bf16 v[8:11], v[200:203], v[184:187], v[8:11]\n\t"
    "s_waitcnt lgkmcnt(2)\n\t"
    "v_mfma_f32_16x16x32_bf16 v[12:15], v[200:203], v[188:191], v[12:15]\n\t"
    "v_mfma_f32_16x16x32_bf16 v[16:19], v[204:207], v[176:179], v[16:19]\n\t"
    "ds_read_b64_tr_b16 v[200:201], v212 offset:45056\n\t"
    "v_mfma_f32_16x16x32_bf16 v[20:23], v[204:207], v[180:183], v[20:23]\n\t"
    "ds_read_b64_tr_b16 v[202:203], v212 offset:45568\n\t"
    "v_mfma_f32_16x16x32_bf16 v[24:27], v[204:207], v[184:187], v[24:27]\n\t"
    "v_mfma_f32_16x16x32_bf16 v[28:31], v[204:207], v[188:191], v[28:31]\n\t"
    "v_mfma_f32_16x16x32_bf16 v[32:35], v[192:195], v[176:179], v[32:35]\n\t"
    "ds_read_b64_tr_b16 v[204:205], v213 offset:45056\n\t"
    "v_mfma_f32_16x16x32_bf16 v[36:39], v[192:195], v[180:183], v[36:39]\n\t"
    "ds_read_b64_tr_b16 v[206:207], v213 offset:45568\n\t"
    "v_mfma_f32_16x16x32_bf16 v[40:43], v[192:195], v[184:187], v[40:43]\n\t"
    "v_mfma_f32_16x16x32_bf16 v[44:47], v[192:195], v[188:191], v[44:47]\n\t"
    "s_waitcnt lgkmcnt(4)\n\t"
    "v_mfma_f32_16x16x32_bf16 v[48:51], v[196:199], v[176:179], v[48:51]\n\t"
    "ds_read_b64_tr_b16 v[192:193], v212 offset:49152\n\t"
    "v_mfma_f32_16x16x32_bf16 v[52:55], v[196:199], v[180:183], v[52:55]\n\t"
    "ds_read_b64_tr_b16 v[194:195], v212 offset:49664\n\t"
    "v_mfma_f32_16x16x32_bf16 v[56:59], v[196:199], v[184:187], v[56:59]\n\t"
    "v_mfma_f32_16x16x32_bf16 v[60:63], v[196:199], v[188:191], v[60:63]\n\t"
    "s_waitcnt lgkmcnt(4)\n\t"
    "v_mfma_f32_16x16x32_bf16 v[64:67], v[200:203], v[176:179], v[64:67]\n\t"
    "ds_read_b64_tr_b16 v[196:197], v213 offset:49152\n\t"
    "v_mfma_f32_16x16x32_bf16 v[68:71], v[200:203], v[180:183], v[68:71]\n\t"
    "ds_read_b64_tr_b16 v[198:199], v213 offset:49664\n\t"
    "v_mfma_f32_16x16x32_bf16 v[72:75], v[200:203], v[184:187], v[72:75]\n\t"
    "v_mfma_f32_16x16x32_bf16 v[76:79], v[200:203], v[188:191], v[76:79]\n\t"
    "s_waitcnt lgkmcnt(4)\n\t"
    "v_mfma_f32_16x16x32_bf16 v[80:83], v[204:207], v[176:179], v[80:83]\n\t"
    "ds_read_b64_tr_b16 v[200:201], v212 offset:53248\n\t"
    "v_mfma_f32_16x16x32_bf16 v[84:87], v[204:207], v[180:183], v[84:87]\n\t"
    "ds_read_b64_tr_b16 v[202:203], v212 offset:53760\n\t"
    "v_mfma_f32_16x16x32_bf16 v[88:91], v[204:207], v[184:187], v[88:91]\n\t"
    "v_mfma_f32_16x16x32_bf16 v[92:95], v[204:207], v[188:191], v[92:95]\n\t"
    "s_barrier\n\t"
    "s_waitcnt lgkmcnt(4)\n\t"
    "v_mfma_f32_16x16x32_bf16 v[96:99], v[192:195], v[176:179], v[96:99]\n\t"
    "ds_read_b64_tr_b16 v[204:205], v213 offset:53248\n\t"
    "v_mfma_f32_16x16x32_bf16 v[100:103], v[192:195], v[180:183], v[100:103]\n\t"
    "ds_read_b64_tr_b16 v[206:207], v213 offset:53760\n\t"
    "v_mfma_f32_16x16x32_bf16 v[104:107], v[192:195], v[184:187], v[104:107]\n\t"
    "ds_read_b128 v[160:163], v208 offset:0\n\t"
    "v_mfma_f32_16x16x32_bf16 v[108:111], v[192:195], v[188:191], v[108:111]\n\t"
    "s_waitcnt lgkmcnt(5)\n\t"
    "v_mfma_f32_16x16x32_bf16 v[112:115], v[196:199], v[176:179], v[112:115]\n\t"
    "ds_read_b64_tr_b16 v[192:193], v209 offset:0\n\t"
    "v_mfma_f32_16x16x32_bf16 v[116:119], v[196:199], v[180:183], v[116:119]\n\t"
    "ds_read_b64_tr_b16 v[194:195], v209 offset:512\n\t"
    "v_mfma_f32_16x16x32_bf16 v[120:123], v[196:199], v[184:187], v[120:123]\n\t"
    "ds_read_b128 v[164:167], v208 offset:1024\n\t"
    "v_mfma_f32_16x16x32_bf16 v[124:127], v[196:199], v[188:191], v[124:127]\n\t"
    "s_waitcnt lgkmcnt(6)\n\t"
    "v_mfma_f32_16x16x32_bf16 v[128:131], v[200:203], v[176:179], v[128:131]\n\t"
    "ds_read_b64_tr_b16 v[196:197], v210 offset:0\n\t"
    "v_mfma_f32_16x16x32_bf16 v[132:135], v[200:203], v[180:183], v[132:135]\n\t"
    "ds_read_b64_tr_b16 v[198:199], v210 offset:512\n\t"
    "v_mfma_f32_16x16x32_bf16 v[136:139], v[200:203], v[184:187], v[136:139]\n\t"
    "ds_read_b128 v[168:171], v208 offset:2048\n\t"
    "v_mfma_f32_16x16x32_bf16 v[140:143], v[200:203], v[188:191], v[140:143]\n\t"
    "s_waitcnt lgkmcnt(7)\n\t"
    "v_mfma_f32_16x16x32_bf16 v[144:147], v[204:207], v[176:179], v[144:147]\n\t"
    "ds_read_b64_tr_b16 v[200:201], v209 offset:4096\n\t"
    "v_mfma_f32_16x16x32_bf16 v[148:151], v[204:207], v[180:183], v[148:151]\n\t"
    "ds_read_b64_tr_b16 v[202:203], v209 offset:4608\n\t"
    "v_mfma_f32_16x16x32_bf16 v[152:155], v[204:207], v[184:187], v[152:155]\n\t"
    "ds_read_b128 v[172:175], v208 offset:3072\n\t"
    "v_mfma_f32_16x16x32_bf16 v[156:159], v[204:207], v[188:191], v[156:159]\n\t"
    "s_sub_u32 %[cnt], %[cnt], 1\n\t"
    "s_cmp_lg_u32 %[cnt], 0\n\t"
    "s_cbranch_scc1 2b\n\t"
    "s_waitcnt lgkmcnt(7)\n\t"
    "v_mfma_f32_16x16x32_bf16 v[0:3], v[192:195], v[160:163], v[0:3]\n\t"
    "ds_read_b64_tr_b16 v[204:205], v210 offset:4096\n\t"
    "s_waitcnt lgkmcnt(7)\n\t"
    "v_mfma_f32_16x16x32_bf16 v[4:7], v[192:195], v[164:167], v[4:7]\n\t"
    "ds_read_b64_tr_b16 v[206:207], v210 offset:4608\n\t"
    "s_waitcnt lgkmcnt(5)\n\t"
    "v_mfma_f32_16x16x32_bf16 v[8:11], v[192:195], v[168:171], v[8:11]\n\t"
    "s_waitcnt lgkmcnt(2)\n\t"
    "v_mfma_f32_16x16x32_bf16 v[12:15], v[192:195], v[172:175], v[12:15]\n\t"
    "v_mfma_f32_16x16x32_bf16 v[16:19], v[196:199], v[160:163], v[16:19]\n\t"
    "ds_read_b64_tr_b16 v[192:193], v209 offset:8192\n\t"
    "v_mfma_f32_16x16x32_bf16 v[20:23], v[196:199], v[164:167], v[20:23]\n\t"
    "ds_read_b64_tr_b16 v[194:195], v209 offset:8704\n\t"
    "v_mfma_f32_16x16x32_bf16 v[24:27], v[196:199], v[168:171], v[24:27]\n\t"
    "v_mfma_f32_16x16x32_bf16 v[28:31], v[196:199], v[172:175], v[28:31]\n\t"
    "buffer_load_dword v216, v217, %[rcode], 0 offen\n\t"
    "buffer_load_dword v216, v218, %[rcode], 0 offen\n\t"
    "v_mfma_f32_16x16x32_bf16 v[32:35], v[200:203], v[160:163], v[32:35]\n\t"
    "ds_read_b64_tr_b16 v[196:197], v210 offset:8192\n\t"
    "v_mfma_f32_16x16x32_bf16 v[36:39], v[200:203], v[164:167], v[36:39]\n\t"
    "ds_read_b64_tr_b16 v[198:199], v210 offset:8704\n\t"
    "v_mfma_f32_16x16x32_bf16 v[40:43], v[200:203], v[168:171], v[40:43]\n\t"
    "v_mfma_f32_16x16x32_bf16 v[44:47], v[200:203], v[172:175], v[44:47]\n\t"
    "s_waitcnt lgkmcnt(4)\n\t"
    "v_mfma_f32_16x16x32_bf16 v[48:51], v[204:207], v[160:163], v[48:51]\n\t"
    "ds_read_b64_tr_b16 v[200:201], v209 offset:12288\n\t"
    "v_mfma_f32_16x16x32_bf16 v[52:55], v[204:207], v[164:167], v[52:55]\n\t"
    "ds_read_b64_tr_b16 v[202:203], v209 offset:12800\n\t"
    "v_mfma_f32_16x16x32_bf16 v[56:59], v[204:207], v[168:171], v[56:59]\n\t"
    "v_mfma_f32_16x16x32_bf16 v[60:63], v[204:207], v[172:175], v[60:63]\n\t"
    "s_waitcnt lgkmcnt(4)\n\t"
    "v_mfma_f32_16x16x32_bf16 v[64:67], v[192:195], v[160:163], v[64:67]\n\t"
    "ds_read_b64_tr_b16 v[204:205], v210 offset:12288\n\t"
    "v_mfma_f32_16x16x32_bf16 v[68:71], v[192:195], v[164:167], v[68:71]\n\t"
    "ds_read_b64_tr_b16 v[206:207], v210 offset:12800\n\t"
    "v_mfma_f32_16x16x32_bf16 v[72:75], v[192:195], v[168:171], v[72:75]\n\t"
    "v_mfma_f32_16x16x32_bf16 v[76:79], v[192:195], v[172:175], v[76:79]\n\t"
    "s_waitcnt lgkmcnt(4)\n\t"
    "v_mfma_f32_16x16x32_bf16 v[80:83], v[196:199], v[160:163], v[80:83]\n\t"
    "ds_read_b64_tr_b16 v[192:193], v209 offset:16384\n\t"
    "v_mfma_f32_16x16x32_bf16 v[84:87], v[196:199], v[164:167], v[84:87]\n\t"
    "ds_read_b64_tr_b16 v[194:195], v209 offset:16896\n\t"
    "v_mfma_f32_16x16x32_bf16 v[88:91], v[196:199], v[168:171], v[88:91]\n\t"
    "v_mfma_f32_16x16x32_bf16 v[92:95], v[196:199], v[172:175], v[92:95]\n\t"
    "s_barrier\n\t"
    "s_waitcnt lgkmcnt(4)\n\t"
    "v_mfma_f32_16x16x32_bf16 v[96:99], v[200:203], v[160:163], v[96:99]\n\t"
    "ds_read_b64_tr_b16 v[196:197], v210 offset:16384\n\t"
    "v_mfma_f32_16x16x32_bf16 v[100:103], v[200:203], v[164:167], v[100:103]\n\t"
    "ds_read_b64_tr_b16 v[198:199], v210 offset:16896\n\t"
    "v_mfma_f32_16x16x32_bf16 v[104:107], v[200:203], v[168:171], v[104:107]\n\t"
    "ds_read_b128 v[176:179], v208 offset:36864\n\t"
    "v_mfma_f32_16x16x32_bf16 v[108:111], v[200:203], v[172:175], v[108:111]\n\t"
    "s_waitcnt lgkmcnt(5)\n\t"
    "v_mfma_f32_16x16x32_bf16 v[112:115], v[204:207], v[160:163], v[112:115]\n\t"
    "ds_read_b64_tr_b16 v[200:201], v209 offset:36864\n\t"
    "v_mfma_f32_16x16x32_bf16 v[116:119], v[204:207], v[164:167], v[116:119]\n\t"
    "ds_read_b64_tr_b16 v[202:203], v209 offset:37376\n\t"
    "v_mfma_f32_16x16x32_bf16 v[120:123], v[204:207], v[168:171], v[120:123]\n\t"
    "ds_read_b128 v[180:183], v208 offset:37888\n\t"
    "v_mfma_f32_16x16x32_bf16 v[124:127], v[204:207], v[172:175], v[124:127]\n\t"
    "s_waitcnt lgkmcnt(6)\n\t"
    "v_mfma_f32_16x16x32_bf16 v[128:131], v[192:195], v[160:163], v[128:131]\n\t"
    "ds_read_b64_tr_b16 v[204:205], v210 offset:36864\n\t"
    "v_mfma_f32_16x16x32_bf16 v[132:135], v[192:195], v[164:167], v[132:135]\n\t"
    "ds_read_b64_tr_b16 v[206:207], v210 offset:37376\n\t"
    "v_mfma_f32_16x16x32_bf16 v[136:139], v[192:195], v[168:171], v[136:139]\n\t"
    "ds_read_b128 v[184:187], v208 offset:38912\n\t"
    "v_mfma_f32_16x16x32_bf16 v[140:143], v[192:195], v[172:175], v[140:143]\n\t"
    "s_waitcnt lgkmcnt(7)\n\t"
    "v_mfma_f32_16x16x32_bf16 v[144:147], v[196:199], v[160:163], v[144:147]\n\t"
    "ds_read_b64_tr_b16 v[192:193], v209 offset:40960\n\t"
    "v_mfma_f32_16x16x32_bf16 v[148:151], v[196:199], v[164:167], v[148:151]\n\t"
    "ds_read_b64_tr_b16 v[194:195], v209 offset:41472\n\t"
    "v_mfma_f32_16x16x32_bf16 v[152:155], v[196:199], v[168:171], v[152:155]\n\t"
    "ds_read_b128 v[188:191], v208 offset:39936\n\t"
    "v_mfma_f32_16x16x32_bf16 v[156:159], v[196:199], v[172:175], v[156:159]\n\t"
    "s_waitcnt lgkmcnt(7)\n\t"
    "v_mfma_f32_16x16x32_bf16 v[0:3], v[200:203], v[176:179], v[0:3]\n\t"
    "ds_read_b64_tr_b16 v[196:197], v210 offset:40960\n\t"
    "s_waitcnt lgkmcnt(7)\n\t"
    "v_mfma_f32_16x16x32_bf16 v[4:7], v[200:203], v[180:183], v[4:7]\n\t"
    "ds_read_b64_tr_b16 v[198:199], v210 offset:41472\n\t"
    "s_waitcnt lgkmcnt(5)\n\t"
    "v_mfma_f32_16x16x32_bf16 v[8:11], v[200:203], v[184:187], v[8:11]\n\t"
    "s_waitcnt lgkmcnt(2)\n\t"
    "v_mfma_f32_16x16x32_bf16 v[12:15], v[200:203], v[188:191], v[12:15]\n\t"
    "v_mfma_f32_16x16x32_bf16 v[16:19], v[204:207], v[176:179], v[16:19]\n\t"
    "ds_read_b64_tr_b16 v[200:201], v209 offset:45056\n\t"
    "v_mfma_f32_16x16x32_bf16 v[20:23], v[204:207], v[180:183], v[20:23]\n\t"
    "ds_read_b64_tr_b16 v[202:203], v209 offset:45568\n\t"
    "v_mfma_f32_16x16x32_bf16 v[24:27], v[204:207], v[184:187], v[24:27]\n\t"
    "v_mfma_f32_16x16x32_bf16 v[28:31], v[204:207], v[188:191], v[28:31]\n\t"
    "v_mfma_f32_16x16x32_bf16 v[32:35], v[192:195], v[176:179], v[32:35]\n\t"
    "ds_read_b64_tr_b16 v[204:205], v210 offset:45056\n\t"
    "v_mfma_f32_16x16x32_bf16 v[36:39], v[192:195], v[180:183], v[36:39]\n\t"
    "ds_read_b64_tr_b16 v[206:207], v210 offset:45568\n\t"
    "v_mfma_f32_16x16x32_bf16 v[40:43], v[192:195], v[184:187], v[40:43]\n\t"
    "v_mfma_f32_16x16x32_bf16 v[44:47], v[192:195], v[188:191], v[44:47]\n\t"
    "s_waitcnt lgkmcnt(4)\n\t"
    "v_mfma_f32_16x16x32_bf16 v[48:51], v[196:199], v[176:179], v[48:51]\n\t"
    "ds_read_b64_tr_b16 v[192:193], v209 offset:49152\n\t"
    "v_mfma_f32_16x16x32_bf16 v[52:55], v[196:199], v[180:183], v[52:55]\n\t"
    "ds_read_b64_tr_b16 v[194:195], v209 offset:49664\n\t"
    "v_mfma_f32_16x16x32_bf16 v[56:59], v[196:199], v[184:187], v[56:59]\n\t"
    "v_mfma_f32_16x16x32_bf16 v[60:63], v[196:199], v[188:191], v[60:63]\n\t"
    "s_waitcnt lgkmcnt(4)\n\t"
    "v_mfma_f32_16x16x32_bf16 v[64:67], v[200:203], v[176:179], v[64:67]\n\t"
    "ds_read_b64_tr_b16 v[196:197], v210 offset:49152\n\t"
    "v_mfma_f32_16x16x32_bf16 v[68:71], v[200:203], v[180:183], v[68:71]\n\t"
    "ds_read_b64_tr_b16 v[198:199], v210 offset:49664\n\t"
    "v_mfma_f32_16x16x32_bf16 v[72:75], v[200:203], v[184:187], v[72:75]\n\t"
    "v_mfma_f32_16x16x32_bf16 v[76:79], v[200:203], v[188:191], v[76:79]\n\t"
    "s_waitcnt lgkmcnt(4)\n\t"
    "v_mfma_f32_16x16x32_bf16 v[80:83], v[204:207], v[176:179], v[80:83]\n\t"
    "ds_read_b64_tr_b16 v[200:201], v209 offset:53248\n\t"
    "v_mfma_f32_16x16x32_bf16 v[84:87], v[204:207], v[180:183], v[84:87]\n\t"
    "ds_read_b64_tr_b16 v[202:203], v209 offset:53760\n\t"
    "v_mfma_f32_16x16x32_bf16 v[88:91], v[204:207], v[184:187], v[88:91]\n\t"
    "v_mfma_f32_16x16x32_bf16 v[92:95], v[204:207], v[188:191], v[92:95]\n\t"
    "s_barrier\n\t"
    "s_waitcnt lgkmcnt(4)\n\t"
    "v_mfma_f32_16x16x32_bf16 v[96:99], v[192:195], v[176:179], v[96:99]\n\t"
    "ds_read_b64_tr_b16 v[204:205], v210 offset:53248\n\t"
    "v_mfma_f32_16x16x32_bf16 v[100:103], v[192:195], v[180:183], v[100:103]\n\t"
    "ds_read_b64_tr_b16 v[206:207], v210 offset:53760\n\t"
    "v_mfma_f32_16x16x32_bf16 v[104:107], v[192:195], v[184:187], v[104:107]\n\t"
    "ds_read_b128 v[160:163], v211 offset:0\n\t"
    "v_mfma_f32_16x16x32_bf16 v[108:111], v[192:195], v[188:191], v[108:111]\n\t"
    "s_waitcnt lgkmcnt(5)\n\t"
    "v_mfma_f32_16x16x32_bf16 v[112:115], v[196:199], v[176:179], v[112:115]\n\t"
    "ds_read_b64_tr_b16 v[192:193], v212 offset:0\n\t"
    "v_mfma_f32_16x16x32_bf16 v[116:119], v[196:199], v[180:183], v[116:119]\n\t"
    "ds_read_b64_tr_b16 v[194:195], v212 offset:512\n\t"
    "v_mfma_f32_16x16x32_bf16 v[120:123], v[196:199], v[184:187], v[120:123]\n\t"
    "ds_read_b128 v[164:167], v211 offset:1024\n\t"
    "v_mfma_f32_16x16x32_bf16 v[124:127], v[196:199], v[188:191], v[124:127]\n\t"
    "s_waitcnt lgkmcnt(6)\n\t"
    "v_mfma_f32_16x16x32_bf16 v[128:131], v[200:203], v[176:179], v[128:131]\n\t"
    "ds_read_b64_tr_b16 v[196:197], v213 offset:0\n\t"
    "v_mfma_f32_16x16x32_bf16 v[132:135], v[200:203], v[180:183], v[132:135]\n\t"
    "ds_read_b64_tr_b16 v[198:199], v213 offset:512\n\t"
    "v_mfma_f32_16x16x32_bf16 v[136:139], v[200:203], v[184:187], v[136:139]\n\t"
    "ds_read_b128 v[168:171], v211 offset:2048\n\t"
    "v_mfma_f32_16x16x32_bf16 v[140:143], v[200:203], v[188:191], v[140:143]\n\t"
    "s_waitcnt lgkmcnt(7)\n\t"
    "v_mfma_f32_16x16x32_bf16 v[144:147], v[204:207], v[176:179], v[144:147]\n\t"
    "ds_read_b64_tr_b16 v[200:201], v212 offset:4096\n\t"
    "v_mfma_f32_16x16x32_bf16 v[148:151], v[204:207], v[180:183], v[148:151]\n\t"
    "ds_read_b64_tr_b16 v[202:203], v212 offset:4608\n\t"
    "v_mfma_f32_16x16x32_bf16 v[152:155], v[204:207], v[184:187], v[152:155]\n\t"
    "ds_read_b128 v[172:175], v211 offset:3072\n\t"
    "v_mfma_f32_16x16x32_bf16 v[156:159], v[204:207], v[188:191], v[156:159]\n\t"
    "s_waitcnt lgkmcnt(7)\n\t"
    "v_mfma_f32_16x16x32_bf16 v[0:3], v[192:195], v[160:163], v[0:3]\n\t"
    "ds_read_b64_tr_b16 v[204:205], v213 offset:4096\n\t"
    "s_waitcnt lgkmcnt(7)\n\t"
    "v_mfma_f32_16x16x32_bf16 v[4:7], v[192:195], v[164:167], v[4:7]\n\t"
    "ds_read_b64_tr_b16 v[206:207], v213 offset:4608\n\t"
    "s_waitcnt lgkmcnt(5)\n\t"
    "v_mfma_f32_16x16x32_bf16 v[8:11], v[192:195], v[168:171], v[8:11]\n\t"
    "s_waitcnt lgkmcnt(2)\n\t"
    "v_mfma_f32_16x16x32_bf16 v[12:15], v[192:195], v[172:175], v[12:15]\n\t"
    "v_mfma_f32_16x16x32_bf16 v[16:19], v[196:199], v[160:163], v[16:19]\n\t"
    "ds_read_b64_tr_b16 v[192:193], v212 offset:8192\n\t"
    "v_mfma_f32_16x16x32_bf16 v[20:23], v[196:199], v[164:167], v[20:23]\n\t"
    "ds_read_b64_tr_b16 v[194:195], v212 offset:8704\n\t"
    "v_mfma_f32_16x16x32_bf16 v[24:27], v[196:199], v[168:171], v[24:27]\n\t"
    "v_mfma_f32_16x16x32_bf16 v[28:31], v[196:199], v[172:175], v[28:31]\n\t"
    "v_mfma_f32_16x16x32_bf16 v[32:35], v[200:203], v[160:163], v[32:35]\n\t"
    "ds_read_b64_tr_b16 v[196:197], v213 offset:8192\n\t"
    "v_mfma_f32_16x16x32_bf16 v[36:39], v[200:203], v[164:167], v[36:39]\n\t"
    "ds_read_b64_tr_b16 v[198:199], v213 offset:8704\n\t"
    "v_mfma_f32_16x16x32_bf16 v[40:43], v[200:203], v[168:171], v[40:43]\n\t"
    "v_mfma_f32_16x16x32_bf16 v[44:47], v[200:203], v[172:175], v[44:47]\n\t"
    "s_waitcnt lgkmcnt(4)\n\t"
    "v_mfma_f32_16x16x32_bf16 v[48:51], v[204:207], v[160:163], v[48:51]\n\t"
    "ds_read_b64_tr_b16 v[200:201], v212 offset:12288\n\t"
    "v_mfma_f32_16x16x32_bf16 v[52:55], v[204:207], v[164:167], v[52:55]\n\t"
    "ds_read_b64_tr_b16 v[202:203], v212 offset:12800\n\t"
    "v_mfma_f32_16x16x32_bf16 v[56:59], v[204:207], v[168:171], v[56:59]\n\t"
    "v_mfma_f32_16x16x32_bf16 v[60:63], v[204:207], v[172:175], v[60:63]\n\t"
    "s_waitcnt lgkmcnt(4)\n\t"
    "v_mfma_f32_16x16x32_bf16 v[64:67], v[192:195], v[160:163], v[64:67]\n\t"
    "ds_read_b64_tr_b16 v[204:205], v213 offset:12288\n\t"
    "v_mfma_f32_16x16x32_bf16 v[68:71], v[192:195], v[164:167], v[68:71]\n\t"
    "ds_read_b64_tr_b16 v[206:207], v213 offset:12800\n\t"
    "v_mfma_f32_16x16x32_bf16 v[72:75], v[192:195], v[168:171], v[72:75]\n\t"
    "v_mfma_f32_16x16x32_bf16 v[76:79], v[192:195], v[172:175], v[76:79]\n\t"
    "s_waitcnt lgkmcnt(4)\n\t"
    "v_mfma_f32_16x16x32_bf16 v[80:83], v[196:199], v[160:163], v[80:83]\n\t"
    "ds_read_b64_tr_b16 v[192:193], v212 offset:16384\n\t"
    "v_mfma_f32_16x16x32_bf16 v[84:87], v[196:199], v[164:167], v[84:87]\n\t"
    "ds_read_b64_tr_b16 v[194:195], v212 offset:16896\n\t"
    "v_mfma_f32_16x16x32_bf16 v[88:91], v[196:199], v[168:171], v[88:91]\n\t"
    "v_mfma_f32_16x16x32_bf16 v[92:95], v[196:199], v[172:175], v[92:95]\n\t"
    "s_barrier\n\t"
    "s_waitcnt lgkmcnt(4)\n\t"
    "v_mfma_f32_16x16x32_bf16 v[96:99], v[200:203], v[160:163], v[96:99]\n\t"
    "ds_read_b64_tr_b16 v[196:197], v213 offset:16384\n\t"
    "v_mfma_f32_16x16x32_bf16 v[100:103], v[200:203], v[164:167], v[100:103]\n\t"
    "ds_read_b64_tr_b16 v[198:199], v213 offset:16896\n\t"
    "v_mfma_f32_16x16x32_bf16 v[104:107], v[200:203], v[168:171], v[104:107]\n\t"
    "ds_read_b128 v[176:179], v211 offset:36864\n\t"
    "v_mfma_f32_16x16x32_bf16 v[108:111], v[200:203], v[172:175], v[108:111]\n\t"
    "s_waitcnt lgkmcnt(5)\n\t"
    "v_mfma_f32_16x16x32_bf16 v[112:115], v[204:207], v[160:163], v[112:115]\n\t"
    "ds_read_b64_tr_b16 v[200:201], v212 offset:36864\n\t"
    "v_mfma_f32_16x16x32_bf16 v[116:119], v[204:207], v[164:167], v[116:119]\n\t"
    "ds_read_b64_tr_b16 v[202:203], v212 offset:37376\n\t"
    "v_mfma_f32_16x16x32_bf16 v[120:123], v[204:207], v[168:171], v[120:123]\n\t"
    "ds_read_b128 v[180:183], v211 offset:37888\n\t"
    "v_mfma_f32_16x16x32_bf16 v[124:127], v[204:207], v[172:175], v[124:127]\n\t"
    "s_waitcnt lgkmcnt(6)\n\t"
    "v_mfma_f32_16x16x32_bf16 v[128:131], v[192:195], v[160:163], v[128:131]\n\t"
    "ds_read_b64_tr_b16 v[204:205], v213 offset:36864\n\t"
    "v_mfma_f32_16x16x32_bf16 v[132:135], v[192:195], v[164:167], v[132:135]\n\t"
    "ds_read_b64_tr_b16 v[206:207], v213 offset:37376\n\t"
    "v_mfma_f32_16x16x32_bf16 v[136:139], v[192:195], v[168:171], v[136:139]\n\t"
    "ds_read_b128 v[184:187], v211 offset:38912\n\t"
    "v_mfma_f32_16x16x32_bf16 v[140:143], v[192:195], v[172:175], v[140:143]\n\t"
    "s_waitcnt lgkmcnt(7)\n\t"
    "v_mfma_f32_16x16x32_bf16 v[144:147], v[196:199], v[160:163], v[144:147]\n\t"
    "ds_read_b64_tr_b16 v[192:193], v212 offset:40960\n\t"
    "v_mfma_f32_16x16x32_bf16 v[148:151], v[196:199], v[164:167], v[148:151]\n\t"
    "ds_read_b64_tr_b16 v[194:195], v212 offset:41472\n\t"
    "v_mfma_f32_16x16x32_bf16 v[152:155], v[196:199], v[168:171], v[152:155]\n\t"
    "ds_read_b128 v[188:191], v211 offset:39936\n\t"
    "v_mfma_f32_16x16x32_bf16 v[156:159], v[196:199], v[172:175], v[156:159]\n\t"
    "s_waitcnt lgkmcnt(7)\n\t"
    "v_mfma_f32_16x16x32_bf16 v[0:3], v[200:203], v[176:179], v[0:3]\n\t"
    "ds_read_b64_tr_b16 v[196:197], v213 offset:40960\n\t"
    "s_waitcnt lgkmcnt(7)\n\t"
    "v_mfma_f32_16x16x32_bf16 v[4:7], v[200:203], v[180:183], v[4:7]\n\t"
    "ds_read_b64_tr_b16 v[198:199], v213 offset:41472\n\t"
    "s_waitcnt lgkmcnt(5)\n\t"
    "v_mfma_f32_16x16x32_bf16 v[8:11], v[200:203], v[184:187], v[8:11]\n\t"
    "s_waitcnt lgkmcnt(2)\n\t"
    "v_mfma_f32_16x16x32_bf16 v[12:15], v[200:203], v[188:191], v[12:15]\n\t"
    "v_mfma_f32_16x16x32_bf16 v[16:19], v[204:207], v[176:179], v[16:19]\n\t"
    "ds_read_b64_tr_b16 v[200:201], v212 offset:45056\n\t"
    "v_mfma_f32_16x16x32_bf16 v[20:23], v[204:207], v[180:183], v[20:23]\n\t"
    "ds_read_b64_tr_b16 v[202:203], v212 offset:45568\n\t"
    "v_mfma_f32_16x16x32_bf16 v[24:27], v[204:207], v[184:187], v[24:27]\n\t"
    "v_mfma_f32_16x16x32_bf16 v[28:31], v[204:207], v[188:191], v[28:31]\n\t"
    "v_mfma_f32_16x16x32_bf16 v[32:35], v[192:195], v[176:179], v[32:35]\n\t"
    "ds_read_b64_tr_b16 v[204:205], v213 offset:45056\n\t"
    "v_mfma_f32_16x16x32_bf16 v[36:39], v[192:195], v[180:183], v[36:39]\n\t"
    "ds_read_b64_tr_b16 v[206:207], v213 offset:45568\n\t"
    "v_mfma_f32_16x16x32_bf16 v[40:43], v[192:195], v[184:187], v[40:43]\n\t"
    "v_mfma_f32_16x16x32_bf16 v[44:47], v[192:195], v[188:191], v[44:47]\n\t"
    "s_waitcnt lgkmcnt(4)\n\t"
    "v_mfma_f32_16x16x32_bf16 v[48:51], v[196:199], v[176:179], v[48:51]\n\t"
    "ds_read_b64_tr_b16 v[192:193], v212 offset:49152\n\t"
    "v_mfma_f32_16x16x32_bf16 v[52:55], v[196:199], v[180:183], v[52:55]\n\t"
    "ds_read_b64_tr_b16 v[194:195], v212 offset:49664\n\t"
    "v_mfma_f32_16x16x32_bf16 v[56:59], v[196:199], v[184:187], v[56:59]\n\t"
    "v_mfma_f32_16x16x32_bf16 v[60:63], v[196:199], v[188:191], v[60:63]\n\t"
    "s_waitcnt lgkmcnt(4)\n\t"
    "v_mfma_f32_16x16x32_bf16 v[64:67], v[200:203], v[176:179], v[64:67]\n\t"
    "ds_read_b64_tr_b16 v[196:197], v213 offset:49152\n\t"
    "v_mfma_f32_16x16x32_bf16 v[68:71], v[200:203], v[180:183], v[68:71]\n\t"
    "ds_read_b64_tr_b16 v[198:199], v213 offset:49664\n\t"
    "v_mfma_f32_16x16x32_bf16 v[72:75], v[200:203], v[184:187], v[72:75]\n\t"
    "v_mfma_f32_16x16x32_bf16 v[76:79], v[200:203], v[188:191], v[76:79]\n\t"
    "s_waitcnt lgkmcnt(4)\n\t"
    "v_mfma_f32_16x16x32_bf16 v[80:83], v[204:207], v[176:179], v[80:83]\n\t"
    "ds_read_b64_tr_b16 v[200:201], v212 offset:53248\n\t"
    "v_mfma_f32_16x16x32_bf16 v[84:87], v[204:207], v[180:183], v[84:87]\n\t"
    "ds_read_b64_tr_b16 v[202:203], v212 offset:53760\n\t"
    "v_mfma_f32_16x16x32_bf16 v[88:91], v[204:207], v[184:187], v[88:91]\n\t"
    "v_mfma_f32_16x16x32_bf16 v[92:95], v[204:207], v[188:191], v[92:95]\n\t"
    "s_waitcnt lgkmcnt(4)\n\t"
    "v_mfma_f32_16x16x32_bf16 v[96:99], v[192:195], v[176:179], v[96:99]\n\t"
    "ds_read_b64_tr_b16 v[204:205], v213 offset:53248\n\t"
    "v_mfma_f32_16x16x32_bf16 v[100:103], v[192:195], v[180:183], v[100:103]\n\t"
    "ds_read_b64_tr_b16 v[206:207], v213 offset:53760\n\t"
    "v_mfma_f32_16x16x32_bf16 v[104:107], v[192:195], v[184:187], v[104:107]\n\t"
    "v_mfma_f32_16x16x32_bf16 v[108:111], v[192:195], v[188:191], v[108:111]\n\t"
    "s_waitcnt lgkmcnt(4)\n\t"
    "v_mfma_f32_16x16x32_bf16 v[112:115], v[196:199], v[176:179], v[112:115]\n\t"
    "v_mfma_f32_16x16x32_bf16 v[116:119], v[196:199], v[180:183], v[116:119]\n\t"
    "v_mfma_f32_16x16x32_bf16 v[120:123], v[196:199], v[184:187], v[120:123]\n\t"
    "v_mfma_f32_16x16x32_bf16 v[124:127], v[196:199], v[188:191], v[124:127]\n\t"
    "s_waitcnt lgkmcnt(2)\n\t"
    "v_mfma_f32_16x16x32_bf16 v[128:131], v[200:203], v[176:179], v[128:131]\n\t"
    "v_mfma_f32_16x16x32_bf16 v[132:135], v[200:203], v[180:183], v[132:135]\n\t"
    "v_mfma_f32_16x16x32_bf16 v[136:139], v[200:203], v[184:187], v[136:139]\n\t"
    "v_mfma_f32_16x16x32_bf16 v[140:143], v[200:203], v[188:191], v[140:143]\n\t"
    "s_waitcnt lgkmcnt(0)\n\t"
    "v_mfma_f32_16x16x32_bf16 v[144:147], v[204:207], v[176:179], v[144:147]\n\t"
    "v_mfma_f32_16x16x32_bf16 v[148:151], v[204:207], v[180:183], v[148:151]\n\t"
    "v_mfma_f32_16x16x32_bf16 v[152:155], v[204:207], v[184:187], v[152:155]\n\t"
    "v_mfma_f32_16x16x32_bf16 v[156:159], v[204:207], v[188:191], v[156:159]\n\t"
    "s_waitcnt vmcnt(0)\n\t"
    "s_nop 7\n\t"
    "s_nop 7\n\t"
        : "={v[0:15]}"(acc0), "={v[16:31]}"(acc1), "={v[32:47]}"(acc2), "={v[48:63]}"(acc3), "={v[64:79]}"(acc4), "={v[80:95]}"(acc5), "={v[96:111]}"(acc6), "={v[112:127]}"(acc7), "={v[128:143]}"(acc8), "={v[144:159]}"(acc9), "+{v214}"(vPf), [cnt] "=&s"(cnt)
        : "{v208}"(vA[0]), "{v209}"(vC[0][0]), "{v210}"(vC[0][1]), "{v211}"(vA[1]), "{v212}"(vC[1][0]), "{v213}"(vC[1][1]), "{v215}"(vPo), "{v217}"(vCd0), "{v218}"(vCd1), [rpd] "s"(rpd), [rpo] "s"(rpo), [rcode] "s"(rcode)
        : "memory", "scc", "v160", "v161", "v162", "v163", "v164", "v165", "v166", "v167", "v168", "v169", "v170", "v171", "v172", "v173", "v174", "v175", "v176", "v177", "v178", "v179", "v180", "v181", "v182", "v183", "v184", "v185", "v186", "v187", "v188", "v189", "v190", "v191", "v192", "v193", "v194", "v195", "v196", "v197", "v198", "v199", "v200", "v201", "v202", "v203", "v204", "v205", "v206", "v207", "v216");
    }

    STAMP(1, 4);
    STAMPC(1, 3);
    asm volatile(".Lfk_mid:");
    asm volatile("" : "+s"(bb));
    kh = bb & 1;
    R = ((bb & 7) >> 1) * 4 + ((bb >> 3) & 3);
    {
      const int ct = bb >> 5;
  lcA = 2 * ct; lcB = 2 * ct + 1;
    }
    constexpr int STORE_AUX = 16;
    const int mt0 = rb * 16 + wm * 4;
    if (!typeB) {
      const int part0 = 2 * ta + wn;
      __builtin_amdgcn_raw_buffer_store_b128((u32x4){pk2(acc8[0], acc8[1]), pk2(acc8[2], acc8[3]), pk2(acc9[0], acc9[1]), pk2(acc9[2], acc9[3])}, rqo, (unsigned)(part0 * 128 + mt0 + 0) * 1024u + lane * 16u, 0, STORE_AUX);
      __builtin_amdgcn_raw_buffer_store_b128((u32x4){pk2(acc8[4], acc8[5]), pk2(acc8[6], acc8[7]), pk2(acc9[4], acc9[5]), pk2(acc9[6], acc9[7])}, rqo, (unsigned)(part0 * 128 + mt0 + 1) * 1024u + lane * 16u, 0, STORE_AUX);
      __builtin_amdgcn_raw_buffer_store_b128((u32x4){pk2(acc8[8], acc8[9]), pk2(acc8[10], acc8[11]), pk2(acc9[8], acc9[9]), pk2(acc9[10], acc9[11])}, rqo, (unsigned)(part0 * 128 + mt0 + 2) * 1024u + lane * 16u, 0, STORE_AUX);
      __builtin_amdgcn_raw_buffer_store_b128((u32x4){pk2(acc8[12], acc8[13]), pk2(acc8[14], acc8[15]), pk2(acc9[12], acc9[13]), pk2(acc9[14], acc9[15])}, rqo, (unsigned)(part0 * 128 + mt0 + 3) * 1024u + lane * 16u, 0, STORE_AUX);
    } else {
      const int part0 = 2 * (24 + 3 * xcd) + wn;
      __builtin_amdgcn_raw_buffer_store_b128((u32x4){pk2(acc4[0], acc4[1]), pk2(acc4[2], acc4[3]), pk2(acc5[0], acc5[1]), pk2(acc5[2], acc5[3])}, rqo, (unsigned)((part0 + 0) * 128 + mt0 + 0) * 1024u + lane * 16u, 0, STORE_AUX);
      __builtin_amdgcn_raw_buffer_store_b128((u32x4){pk2(acc6[0], acc6[1]), pk2(acc6[2], acc6[3]), pk2(acc7[0], acc7[1]), pk2(acc7[2], acc7[3])}, rqo, (unsigned)((part0 + 2) * 128 + mt0 + 0) * 1024u + lane * 16u, 0, STORE_AUX);
      __builtin_amdgcn_raw_buffer_store_b128((u32x4){pk2(acc8[0], acc8[1]), pk2(acc8[2], acc8[3]), pk2(acc9[0], acc9[1]), pk2(acc9[2], acc9[3])}, rqo, (unsigned)((part0 + 4) * 128 + mt0 + 0) * 1024u + lane * 16u, 0, STORE_AUX);
      __builtin_amdgcn_raw_buffer_store_b128((u32x4){pk2(acc4[4], acc4[5]), pk2(acc4[6], acc4[7]), pk2(acc5[4], acc5[5]), pk2(acc5[6], acc5[7])}, rqo, (unsigned)((part0 + 0) * 128 + mt0 + 1) * 1024u + lane * 16u, 0, STORE_AUX);
      __builtin_amdgcn_raw_buffer_store_b128((u32x4){pk2(acc6[4], acc6[5]), pk2(acc6[6], acc6[7]), pk2(acc7[4], acc7[5]), pk2(acc7[6], acc7[7])}, rqo, (unsigned)((part0 + 2) * 128 + mt0 + 1) * 1024u + lane * 16u, 0, STORE_AUX);
      __builtin_amdgcn_raw_buffer_store_b128((u32x4){pk2(acc8[4], acc8[5]), pk2(acc8[6], acc8[7]), pk2(acc9[4], acc9[5]), pk2(acc9[6], acc9[7])}, rqo, (unsigned)((part0 + 4) * 128 + mt0 + 1) * 1024u + lane * 16u, 0, STORE_AUX);
      __builtin_amdgcn_raw_buffer_store_b128((u32x4){pk2(acc4[8], acc4[9]), pk2(acc4[10], acc4[11]), pk2(acc5[8], acc5[9]), pk2(acc5[10], acc5[11])}, rqo, (unsigned)((part0 + 0) * 128 + mt0 + 2) * 1024u + lane * 16u, 0, STORE_AUX);
      __builtin_amdgcn_raw_buffer_store_b128((u32x4){pk2(acc6[8], acc6[9]), pk2(acc6[10], acc6[11]), pk2(acc7[8], acc7[9]), pk2(acc7[10], acc7[11])}, rqo, (unsigned)((part0 + 2) * 128 + mt0 + 2) * 1024u + lane * 16u, 0, STORE_AUX);
      __builtin_amdgcn_raw_buffer_store_b128((u32x4){pk2(acc8[8], acc8[9]), pk2(acc8[10], acc8[11]), pk2(acc9[8], acc9[9]), pk2(acc9[10], acc9[11])}, rqo, (unsigned)((part0 + 4) * 128 + mt0 + 2) * 1024u + lane * 16u, 0, STORE_AUX);
      __builtin_amdgcn_raw_buffer_store_b128((u32x4){pk2(acc4[12], acc4[13]), pk2(acc4[14], acc4[15]), pk2(acc5[12], acc5[13]), pk2(acc5[14], acc5[15])}, rqo, (unsigned)((part0 + 0) * 128 + mt0 + 3) * 1024u + lane * 16u, 0, STORE_AUX);
      __builtin_amdgcn_raw_buffer_store_b128((u32x4){pk2(acc6[12], acc6[13]), pk2(acc6[14], acc6[15]), pk2(acc7[12], acc7[13]), pk2(acc7[14], acc7[15])}, rqo, (unsigned)((part0 + 2) * 128 + mt0 + 3) * 1024u + lane * 16u, 0, STORE_AUX);
      __builtin_amdgcn_raw_buffer_store_b128((u32x4){pk2(acc8[12], acc8[13]), pk2(acc8[14], acc8[15]), pk2(acc9[12], acc9[13]), pk2(acc9[14], acc9[15])}, rqo, (unsigned)((part0 + 4) * 128 + mt0 + 3) * 1024u + lane * 16u, 0, STORE_AUX);
    }
    BAR();
    {
      const i32x4 rw0 = make_srd(wd + (size_t)kh * 56 * 32 * D_, (unsigned)(F_ - kh * 56 * 32) * 2048u);
      unsigned vw[2];
#pragma unroll
      for (int i = 0; i < 2; ++i) {
        const int row = 4 * (w4 + 4 * i) + (lane >> 4);
        const int ch = (lane & 15) ^ ((row & 7) << 1);
        vw[i] = row * 2048 + ((ch & 8) ? lcB : lcA) * 128 + (ch & 7) * 16;
      }
      unsigned sow0 = par * 65536;
      const unsigned swv0 = lb + par * 65536 + w4 * 1024;
      asm volatile(
    "s_add_u32 m0, %[swv], 0\n\t"
    "s_nop 0\n\t"
    "buffer_load_dwordx4 %[vw0], %[rw], %[sow] offen lds\n\t"
    "s_add_u32 m0, %[swv], 4096\n\t"
    "s_nop 0\n\t"
    "buffer_load_dwordx4 %[vw1], %[rw], %[sow] offen lds\n\t"
    "s_add_u32 %[sow], %[sow], 131072\n\t"
    "s_add_u32 m0, %[swv], 8192\n\t"
    "s_nop 0\n\t"
    "buffer_load_dwordx4 %[vw0], %[rw], %[sow] offen lds\n\t"
    "s_add_u32 m0, %[swv], 12288\n\t"
    "s_nop 0\n\t"
    "buffer_load_dwordx4 %[vw1], %[rw], %[sow] offen lds\n\t"
    "s_add_u32 %[sow], %[sow], 131072\n\t"
    "s_add_u32 m0, %[swv], 16384\n\t"
    "s_nop 0\n\t"
    "buffer_load_dwordx4 %[vw0], %[rw], %[sow] offen lds\n\t"
    "s_add_u32 m0, %[swv], 20480\n\t"
    "s_nop 0\n\t"
    "buffer_load_dwordx4 %[vw1], %[rw], %[sow] offen lds\n\t"
    "s_add_u32 %[sow], %[sow], 131072\n\t"
    "s_add_u32 m0, %[swv], 24576\n\t"
    "s_nop 0\n\t"
    "buffer_load_dwordx4 %[vw0], %[rw], %[sow] offen lds\n\t"
    "s_add_u32 m0, %[swv], 28672\n\t"
    "s_nop 0\n\t"
    "buffer_load_dwordx4 %[vw1], %[rw], %[sow] offen lds\n\t"
    "s_add_u32 %[sow], %[sow], 131072\n\t"
          : [sow] "+&s"(sow0)
          : [vw0] "v"(vw[0]), [vw1] "v"(vw[1]), [rw] "s"(rw0), [swv] "s"(swv0)
          : "memory", "scc");
    }
    if (!typeB) {
      const int k0 = 4 * ta + wn;
      hoff = (unsigned)(mt0 * KS2 + (k0 / 56) * 72 + k0 % 56) * 1024u + lane * 16u;
      hq[0] = (u32x4){pk2(acc0[0] * acc4[0], acc0[1] * acc4[1]), pk2(acc0[2] * acc4[2], acc0[3] * acc4[3]), pk2(acc1[0] * acc5[0], acc1[1] * acc5[1]), pk2(acc1[2] * acc5[2], acc1[3] * acc5[3])};
      hq[1] = (u32x4){pk2(acc2[0] * acc6[0], acc2[1] * acc6[1]), pk2(acc2[2] * acc6[2], acc2[3] * acc6[3]), pk2(acc3[0] * acc7[0], acc3[1] * acc7[1]), pk2(acc3[2] * acc7[2], acc3[3] * acc7[3])};
      hq[2] = (u32x4){pk2(acc0[4] * acc4[4], acc0[5] * acc4[5]), pk2(acc0[6] * acc4[6], acc0[7] * acc4[7]), pk2(acc1[4] * acc5[4], acc1[5] * acc5[5]), pk2(acc1[6] * acc5[6], acc1[7] * acc5[7])};
      hq[3] = (u32x4){pk2(acc2[4] * acc6[4], acc2[5] * acc6[5]), pk2(acc2[6] * acc6[6], acc2[7] * acc6[7]), pk2(acc3[4] * acc7[4], acc3[5] * acc7[5]), pk2(acc3[6] * acc7[6], acc3[7] * acc7[7])};
      hq[4] = (u32x4){pk2(acc0[8] * acc4[8], acc0[9] * acc4[9]), pk2(acc0[10] * acc4[10], acc0[11] * acc4[11]), pk2(acc1[8] * acc5[8], acc1[9] * acc5[9]), pk2(acc1[10] * acc5[10], acc1[11] * acc5[11])};
      hq[5] = (u32x4){pk2(acc2[8] * acc6[8], acc2[9] * acc6[9]), pk2(acc2[10] * acc6[10], acc2[11] * acc6[11]), pk2(acc3[8] * acc7[8], acc3[9] * acc7[9]), pk2(acc3[10] * acc7[10], acc3[11] * acc7[11])};
      hq[6] = (u32x4){pk2(acc0[12] * acc4[12], acc0[13] * acc4[13]), pk2(acc0[14] * acc4[14], acc0[15] * acc4[15]), pk2(acc1[12] * acc5[12], acc1[13] * acc5[13]), pk2(acc1[14] * acc5[14], acc1[15] * acc5[15])};
      hq[7] = (u32x4){pk2(acc2[12] * acc6[12], acc2[13] * acc6[13]), pk2(acc2[14] * acc6[14], acc2[15] * acc6[15]), pk2(acc3[12] * acc7[12], acc3[13] * acc7[13]), pk2(acc3[14] * acc7[14], acc3[15] * acc7[15])};
    } else {
      const int k0 = 96 + 2 * xcd + wn;
      hoff = (unsigned)(mt0 * KS2 + 72 + (k0 - 56)) * 1024u + lane * 16u;
      hq[0] = (u32x4){pk2(acc0[0] * acc2[0], acc0[1] * acc2[1]), pk2(acc0[2] * acc2[2], acc0[3] * acc2[3]), pk2(acc1[0] * acc3[0], acc1[1] * acc3[1]), pk2(acc1[2] * acc3[2], acc1[3] * acc3[3])};
      hq[4] = hq[0];
      hq[1] = (u32x4){pk2(acc0[4] * acc2[4], acc0[5] * acc2[5]), pk2(acc0[6] * acc2[6], acc0[7] * acc2[7]), pk2(acc1[4] * acc3[4], acc1[5] * acc3[5]), pk2(acc1[6] * acc3[6], acc1[7] * acc3[7])};
      hq[5] = hq[1];
      hq[2] = (u32x4){pk2(acc0[8] * acc2[8], acc0[9] * acc2[9]), pk2(acc0[10] * acc2[10], acc0[11] * acc2[11]), pk2(acc1[8] * acc3[8], acc1[9] * acc3[9]), pk2(acc1[10] * acc3[10], acc1[11] * acc3[11])};
      hq[6] = hq[2];
      hq[3] = (u32x4){pk2(acc0[12] * acc2[12], acc0[13] * acc2[13]), pk2(acc0[14] * acc2[14], acc0[15] * acc2[15]), pk2(acc1[12] * acc3[12], acc1[13] * acc3[13]), pk2(acc1[14] * acc3[14], acc1[15] * acc3[15])};
      hq[7] = hq[3];
    }
    if (tid == 0) *(unsigned*)(smem + LC) = 0u;
    asm volatile("s_waitcnt vmcnt(8)" ::: "memory");
    BAR();
    if (tid == 0) st_word(sy, SY_F + b * 128 + F_Q, tag);
  }
#define H_STORE(m) do { asm volatile("" ::: "memory"); \
    if (!typeB) { __builtin_amdgcn_raw_buffer_store_b128(hq[2 * (m)], rho, hoff + ((m) * KS2) * 1024u, 0, 16); \
                  __builtin_amdgcn_raw_buffer_store_b128(hq[2 * (m) + 1], rho, hoff + ((m) * KS2 + 2) * 1024u, 0, 16); } \
    else __builtin_amdgcn_raw_buffer_store_b128(hq[m], rho, hoff + ((m) * KS2) * 1024u, 0, 16); \
    asm volatile("" ::: "memory"); } while (0)
  STAMP(2, 0);
  {
    int tid = threadIdx.x;
    asm volatile("" : "+v"(tid));
    const int lane = tid & 63;
    const int g = lane >> 4, c = lane & 15;
    if (w == 0) {
      const int i5 = lane < 5 ? lane : 0;
      const int pp = (i5 == 0 ? 0 : (i5 == 1 || i5 == 3) ? 32 : 64) + head;
      const int pr = (i5 >= 3 && rb > 0) ? rb - 1 : rb;
      const int cu = pp >> 1;
      const int px = cu < 24 ? cu / 3 : (cu - 24) / 3, ps = cu < 24 ? cu % 3 : 3;
      wait_tag(sy, SY_F + (px + 8 * (ps * 8 + pr)) * 128 + F_Q, tag);
    } else if (w == 1 && b == 0) {
      unsigned spins = 0;
      for (;;) {
        const bool ok = ld_word(sy, SY_R + lane * 8) == tag && ld_word(sy, SY_R + 512 + lane * 8) == tag &&
                        ld_word(sy, SY_R + 1024 + lane * 8) == tag && ld_word(sy, SY_R + 1536 + lane * 8) == tag;
        if (__all(ok) || ++spins > SPIN_MAX) break;
        __builtin_amdgcn_s_sleep(POLL_SLEEP);
      }
      if (lane == 0) st_word(sy, SY_EP, epoch);
    }
    BAR();
    STAMP(2, 2);
    const int t0 = rb * 16 - 16;
    u32x4 kr[4], vr[4];
#pragma unroll
    for (int i = 0; i < 4; ++i) {
      const int t = t0 + i * 8 + w, tc = t < 0 ? 0 : t;
      kr[i] = __builtin_amdgcn_raw_buffer_load_b128(rqo, (unsigned)(((32 + head) * 128 + tc) * 1024) + lane * 16u, 0, 16);
      vr[i] = __builtin_amdgcn_raw_buffer_load_b128(rqo, (unsigned)(((64 + head) * 128 + tc) * 1024) + lane * 16u, 0, 16);
    }
    bf16x8 qf[2];
#pragma unroll
    for (int i = 0; i < 2; ++i)
      qf[i] = __builtin_bit_cast(bf16x8, __builtin_amdgcn_raw_buffer_load_b128(rqo, (unsigned)((head * 128 + rb * 16 + 2 * w + i) * 1024) + lane * 16u, 0, 16));
#pragma unroll
    for (int i = 0; i < 4; ++i) {
      const int t = i * 8 + w;
      *(u32x4*)(ksm + t * 1024 + lane * 16) = kr[i];
      u32x4 v = vr[i];
      if ((c >> 2) & 1) v = (u32x4){v[2], v[3], v[0], v[1]};
      *(u32x4*)(vsm + (t * 16 + c) * 64 + g * 16) = v;
    }
    BAR();
    STAMP(2, 4);
    f32x4 s[2][18];
#pragma unroll
    for (int kt = 0; kt < 18; ++kt) {
      const bf16x8 kf = *(const bf16x8*)(ksm + (2 * w + kt) * 1024 + lane * 16);
      if (kt < 17) s[0][kt] = MFMA16(kf, qf[0], ((f32x4){0.f, 0.f, 0.f, 0.f}));
      if (kt > 0) s[1][kt] = MFMA16(kf, qf[1], ((f32x4){0.f, 0.f, 0.f, 0.f}));
    }
    asm volatile("" ::: "memory");
    if (!typeB) {
#pragma unroll
      for (int m = 0; m < 4; ++m) {
        __builtin_amdgcn_raw_buffer_store_b128(hq[2 * m], rho, hoff + (m * KS2) * 1024u, 0, 16);
        __builtin_amdgcn_raw_buffer_store_b128(hq[2 * m + 1], rho, hoff + (m * KS2 + 2) * 1024u, 0, 16);
      }
    } else {
#pragma unroll
      for (int m = 0; m < 4; ++m) __builtin_amdgcn_raw_buffer_store_b128(hq[m], rho, hoff + (m * KS2) * 1024u, 0, 16);
    }
    asm volatile("" ::: "memory");
    const float NEG = -1e30f;
#pragma unroll
    for (int i = 0; i < 4; ++i) {
      const int r = 4 * g + i;
      s[0][0][i] = r > c ? s[0][0][i] : NEG;
      s[0][16][i] = r <= c ? s[0][16][i] : NEG;
      s[1][1][i] = r > c ? s[1][1][i] : NEG;
      s[1][17][i] = r <= c ? s[1][17][i] : NEG;
    }
    if (rb == 0) {
      asm volatile("" ::: "memory");
#pragma unroll
      for (int kt = 0; kt < 16; ++kt) {
        const bool gone = 2 * w + kt < 16;
#pragma unroll
        for (int i = 0; i < 4; ++i) {
          s[0][kt][i] = gone ? NEG : s[0][kt][i];
          if (kt > 0) s[1][kt][i] = gone ? NEG : s[1][kt][i];
        }
      }
      asm volatile("" ::: "memory");
    }
    const float sc = 0.17677669529663687f * 1.4426950408889634f;
    float nm[2];
#pragma unroll
    for (int qt = 0; qt < 2; ++qt) {
      float mx = max2f(s[qt][qt][0], s[qt][qt][1]), my = max2f(s[qt][qt][2], s[qt][qt][3]);
#pragma unroll
      for (int kt = qt + 1; kt < 17 + qt; ++kt) {
        mx = max3f(mx, s[qt][kt][0], s[qt][kt][1]);
        my = max3f(my, s[qt][kt][2], s[qt][kt][3]);
      }
      mx = max2f(mx, my);
      nm[qt] = -rows_max(mx) * sc;
    }
    f32x4 sum[2] = {};
    f32x4 o[2][2] = {};
    const bf16x8 ones = {0x3f80, 0x3f80, 0x3f80, 0x3f80, 0x3f80, 0x3f80, 0x3f80, 0x3f80};
    const int q = (lane >> 2) & 3, p = lane & 3;
    const unsigned vbase = lb + 32768 + (2 * w * 16 + 4 * g + q) * 64 + p * 16;
#pragma unroll
    for (int pi = 0; pi < 9; ++pi) {
      typedef __attribute__((address_space(3))) bf16x4* lp;
      bf16x8 vf[2];
#pragma unroll
      for (int dt = 0; dt < 2; ++dt) {
        const unsigned ad = vbase + ((dt ^ (g & 1)) * 8) + pi * 2048;
        const bf16x4 lo = __builtin_amdgcn_ds_read_tr16_b64_v4i16((lp)(size_t)ad);
        const bf16x4 hi = __builtin_amdgcn_ds_read_tr16_b64_v4i16((lp)(size_t)(ad + 1024));
        vf[dt] = __builtin_shufflevector(lo, hi, 0, 1, 2, 3, 4, 5, 6, 7);
      }
#pragma unroll
      for (int qt = 0; qt < 2; ++qt) {
        float e[8];
#pragma unroll
        for (int hh = 0; hh < 2; ++hh) {
          const int kt = 2 * pi + hh;
          const bool live = kt >= qt && kt < 17 + qt;
#pragma unroll
          for (int i = 0; i < 4; ++i) e[4 * hh + i] = live ? __builtin_amdgcn_exp2f(__builtin_fmaf(s[qt][kt][i], sc, nm[qt])) : 0.f;
        }
        const u32x4 t = {pk2(e[0], e[1]), pk2(e[2], e[3]), pk2(e[4], e[5]), pk2(e[6], e[7])};
        const bf16x8 pf = __builtin_bit_cast(bf16x8, t);
        o[qt][0] = MFMA16(vf[0], pf, o[qt][0]);
        o[qt][1] = MFMA16(vf[1], pf, o[qt][1]);
        sum[qt] = MFMA16(ones, pf, sum[qt]);
      }
      if (pi == H_SIG_PAIR) {
        typedef __attribute__((address_space(3))) unsigned* lpu;
        DRAIN_VM();
        if (lane == 0) {
          const unsigned prev = __hip_atomic_fetch_add((lpu)(size_t)(lb + LC), 1u, __ATOMIC_RELAXED, __HIP_MEMORY_SCOPE_WORKGROUP);
          if (prev == 7u) {
            st_word(sy, SY_F + b * 128 + F_H, tag);
          }
        }
      }
    }
    const int sl = (head >> 4) * 72 + 56 + (head & 15);
#pragma unroll
    for (int qt = 0; qt < 2; ++qt) {
      const int mt = rb * 16 + 2 * w + qt;
      const float iv = 1.0f / sum[qt][0];
      const u32x4 t = {pk2(o[qt][0][0] * iv, o[qt][0][1] * iv), pk2(o[qt][0][2] * iv, o[qt][0][3] * iv),
                       pk2(o[qt][1][0] * iv, o[qt][1][1] * iv), pk2(o[qt][1][2] * iv, o[qt][1][3] * iv)};
      __builtin_amdgcn_raw_buffer_store_b128(t, rho, (unsigned)(mt * KS2 + sl) * 1024u + lane * 16u, 0, 16);
    }
    STAMP(2, 3);
  }
  STAMP(2, 1);
  {
    int tid = threadIdx.x;
    asm volatile("" : "+v"(tid));
    const int lane = tid & 63;
    const i32x4 ra = make_srd(a2_t + ((size_t)R * 8 * KS2 + kh * 72) * 512, (8u * KS2 - kh * 72u) * 1024u);
    i32x4 rw = make_srd(wd + (size_t)kh * 56 * 32 * D_, (unsigned)(F_ - kh * 56 * 32) * 2048u);
    const i32x4 ro = make_srd(wo + (size_t)kh * 16 * 32 * D_, (unsigned)(D_ - kh * 16 * 32) * 2048u);
    const char* sy3 = sy;
    asm volatile("" : "+s"(sy3));
    const i32x4 rf = make_srd(sy3, SY_BYTES);
    const int g = lane >> 4, q = (lane >> 2) & 3, p = lane & 3;
    const unsigned phi = (4 * g + q) & 7;
    unsigned vW[8], vAs[2], vWs[2];
#pragma unroll
    for (int t = 0; t < 8; ++t) vW[t] = lb + par * 65536 + (4 * g + q) * 256 + ((t ^ phi) * 32) + p * 8;
#pragma unroll
    for (int i = 0; i < 2; ++i) {
      vAs[i] = (2 * w4 + i) * (KS2 * 1024) + lane * 16;
      const int row = 4 * (w4 + 4 * i) + (lane >> 4);
      const int ch = (lane & 15) ^ ((row & 7) << 1);
      vWs[i] = row * 2048 + ((ch & 8) ? lcB : lcA) * 128 + (ch & 7) * 16;
    }
    const unsigned vWd = lb + par * 65536 + w4 * 1024 + lane * 16;
    (void)vWd;
    const int ohead = 16 * kh + (lane & 15);
    const unsigned vFo = SY_F + ((ohead >> 2) + 8 * ((ohead & 3) * 8 + (R >> 1))) * 128 + F_O;
    const unsigned vSig = SY_F + b * 128 + F_O;
    const u32x2 vSig2 = {(unsigned)tag, (unsigned)(tag >> 32)};
    const unsigned tlo = __builtin_amdgcn_readfirstlane((unsigned)tag), thi = __builtin_amdgcn_readfirstlane((unsigned)(tag >> 32));
    const unsigned swave = __builtin_amdgcn_readfirstlane(w);
    unsigned soa = __builtin_amdgcn_readfirstlane(par * 1024), sow = __builtin_amdgcn_readfirstlane(par * 65536 + 4 * 131072), cnt, spin;
    const unsigned soo = __builtin_amdgcn_readfirstlane(par * 65536);
    const unsigned swv = __builtin_amdgcn_readfirstlane(lb + par * 65536 + w4 * 1024);
    const int tok = (R * 8 + 2 * w4 + par) * 16 + (lane & 15);
    const unsigned xo = (unsigned)(tok * D_ + 64 * (kh ? lcB : lcA) + 16 * (g & 1) + 8 * (g >> 1)) * 2u;
    u32x4 xv[2];
    {
      const __amdgpu_buffer_rsrc_t rx = __builtin_amdgcn_make_buffer_rsrc((void*)x, 0, S_ * D_ * 2, 0x00020000);
      xv[0] = __builtin_amdgcn_raw_buffer_load_b128(rx, xo, 0, 0);
      xv[1] = __builtin_amdgcn_raw_buffer_load_b128(rx, xo + 64, 0, 0);
    }
    if (w == 0) {
      const int i = lane < (kh ? 18 : 14) ? lane : 0;
      const int tq = kh ? 14 + i : i;
      const int px = tq < 24 ? tq / 3 : tq - 24, ps = tq < 24 ? tq % 3 : 3;
      wait_tag(sy, SY_F + (px + 8 * (ps * 8 + (R >> 1))) * 128 + F_H, tag);
    }
    BAR();
    STAMP(3, 0);
    STAMPC(3, 2);
    f32x16 acc0, acc1, acc2, acc3;
    asm volatile(
    "buffer_load_dwordx4 v[64:67], v136, %[ra], %[soa] offen sc1\n\t"
    "buffer_load_dwordx4 v[68:71], v137, %[ra], %[soa] offen sc1\n\t"
    "s_add_u32 %[soa], %[soa], 2048\n\t"
    "buffer_load_dwordx4 v[72:75], v136, %[ra], %[soa] offen sc1\n\t"
    "buffer_load_dwordx4 v[76:79], v137, %[ra], %[soa] offen sc1\n\t"
    "s_add_u32 %[soa], %[soa], 2048\n\t"
    "buffer_load_dwordx4 v[80:83], v136, %[ra], %[soa] offen sc1\n\t"
    "buffer_load_dwordx4 v[84:87], v137, %[ra], %[soa] offen sc1\n\t"
    "s_add_u32 %[soa], %[soa], 2048\n\t"
    "s_add_u32 m0, %[swv], 32768\n\t"
    "s_nop 0\n\t"
    "buffer_load_dwordx4 v138, s[40:43], %[sow] offen lds\n\t"
    "s_add_u32 m0, %[swv], 36864\n\t"
    "s_nop 0\n\t"
    "buffer_load_dwordx4 v139, s[40:43], %[sow] offen lds\n\t"
    "s_add_u32 %[sow], %[sow], 131072\n\t"
    "s_add_u32 m0, %[swv], 40960\n\t"
    "s_nop 0\n\t"
    "buffer_load_dwordx4 v138, s[40:43], %[sow] offen lds\n\t"
    "s_add_u32 m0, %[swv], 45056\n\t"
    "s_nop 0\n\t"
    "buffer_load_dwordx4 v139, s[40:43], %[sow] offen lds\n\t"
    "s_add_u32 %[sow], %[sow], 131072\n\t"
    "s_barrier\n\t"
    "ds_read_b64_tr_b16 v[96:97], v128 offset:0\n\t"
    "ds_read_b64_tr_b16 v[98:99], v128 offset:4096\n\t"
    "ds_read_b64_tr_b16 v[100:101], v129 offset:0\n\t"
    "ds_read_b64_tr_b16 v[102:103], v129 offset:4096\n\t"
    "ds_read_b64_tr_b16 v[104:105], v130 offset:0\n\t"
    "ds_read_b64_tr_b16 v[106:107], v130 offset:4096\n\t"
    "ds_read_b64_tr_b16 v[108:109], v131 offset:0\n\t"
    "ds_read_b64_tr_b16 v[110:111], v131 offset:4096\n\t"
    "ds_read_b64_tr_b16 v[112:113], v132 offset:0\n\t"
    "ds_read_b64_tr_b16 v[114:115], v132 offset:4096\n\t"
    "ds_read_b64_tr_b16 v[116:117], v133 offset:0\n\t"
    "ds_read_b64_tr_b16 v[118:119], v133 offset:4096\n\t"
    "s_waitcnt vmcnt(9)\n\t"
    "s_waitcnt lgkmcnt(10)\n\t"
    "v_mfma_f32_16x16x32_bf16 v[0:3], v[96:99], v[64:67], 0\n\t"
    "ds_read_b64_tr_b16 v[120:121], v134 offset:0\n\t"
    "s_waitcnt vmcnt(8)\n\t"
    "v_mfma_f32_16x16x32_bf16 v[4:7], v[96:99], v[68:71], 0\n\t"
    "ds_read_b64_tr_b16 v[122:123], v134 offset:4096\n\t"
    "s_waitcnt lgkmcnt(10)\n\t"
    "v_mfma_f32_16x16x32_bf16 v[8:11], v[100:103], v[64:67], 0\n\t"
    "ds_read_b64_tr_b16 v[124:125], v135 offset:0\n\t"
    "v_mfma_f32_16x16x32_bf16 v[12:15], v[100:103], v[68:71], 0\n\t"
    "ds_read_b64_tr_b16 v[126:127], v135 offset:4096\n\t"
    "s_waitcnt lgkmcnt(10)\n\t"
    "v_mfma_f32_16x16x32_bf16 v[16:19], v[104:107], v[64:67], 0\n\t"
    "ds_read_b64_tr_b16 v[96:97], v128 offset:8192\n\t"
    "s_add_u32 m0, %[swv], 49152\n\t"
    "v_mfma_f32_16x16x32_bf16 v[20:23], v[104:107], v[68:71], 0\n\t"
    "ds_read_b64_tr_b16 v[98:99], v128 offset:12288\n\t"
    "buffer_load_dwordx4 v138, s[40:43], %[sow] offen lds\n\t"
    "s_waitcnt lgkmcnt(10)\n\t"
    "v_mfma_f32_16x16x32_bf16 v[24:27], v[108:111], v[64:67], 0\n\t"
    "ds_read_b64_tr_b16 v[100:101], v129 offset:8192\n\t"
    "v_mfma_f32_16x16x32_bf16 v[28:31], v[108:111], v[68:71], 0\n\t"
    "ds_read_b64_tr_b16 v[102:103], v129 offset:12288\n\t"
    "s_waitcnt lgkmcnt(10)\n\t"
    "v_mfma_f32_16x16x32_bf16 v[32:35], v[112:115], v[64:67], 0\n\t"
    "ds_read_b64_tr_b16 v[104:105], v130 offset:8192\n\t"
    "s_add_u32 m0, %[swv], 53248\n\t"
    "v_mfma_f32_16x16x32_bf16 v[36:39], v[112:115], v[68:71], 0\n\t"
    "ds_read_b64_tr_b16 v[106:107], v130 offset:12288\n\t"
    "buffer_load_dwordx4 v139, s[40:43], %[sow] offen lds\n\t"
    "s_add_u32 %[sow], %[sow], 131072\n\t"
    "s_waitcnt lgkmcnt(10)\n\t"
    "v_mfma_f32_16x16x32_bf16 v[40:43], v[116:119], v[64:67], 0\n\t"
    "ds_read_b64_tr_b16 v[108:109], v131 offset:8192\n\t"
    "v_mfma_f32_16x16x32_bf16 v[44:47], v[116:119], v[68:71], 0\n\t"
    "ds_read_b64_tr_b16 v[110:111], v131 offset:12288\n\t"
    "buffer_load_dwordx4 v[88:91], v136, %[ra], %[soa] offen sc1\n\t"
    "s_waitcnt lgkmcnt(10)\n\t"
    "v_mfma_f32_16x16x32_bf16 v[48:51], v[120:123], v[64:67], 0\n\t"
    "ds_read_b64_tr_b16 v[112:113], v132 offset:8192\n\t"
    "v_mfma_f32_16x16x32_bf16 v[52:55], v[120:123], v[68:71], 0\n\t"
    "ds_read_b64_tr_b16 v[114:115], v132 offset:12288\n\t"
    "s_waitcnt lgkmcnt(10)\n\t"
    "v_mfma_f32_16x16x32_bf16 v[56:59], v[124:127], v[64:67], 0\n\t"
    "ds_read_b64_tr_b16 v[116:117], v133 offset:8192\n\t"
    "v_mfma_f32_16x16x32_bf16 v[60:63], v[124:127], v[68:71], 0\n\t"
    "ds_read_b64_tr_b16 v[118:119], v133 offset:12288\n\t"
    "buffer_load_dwordx4 v[92:95], v137, %[ra], %[soa] offen sc1\n\t"
    "s_add_u32 %[soa], %[soa], 2048\n\t"
    "s_waitcnt vmcnt(11)\n\t"
    "s_waitcnt lgkmcnt(10)\n\t"
    "v_mfma_f32_16x16x32_bf16 v[0:3], v[96:99], v[72:75], v[0:3]\n\t"
    "ds_read_b64_tr_b16 v[120:121], v134 offset:8192\n\t"
    "s_waitcnt vmcnt(10)\n\t"
    "v_mfma_f32_16x16x32_bf16 v[4:7], v[96:99], v[76:79], v[4:7]\n\t"
    "ds_read_b64_tr_b16 v[122:123], v134 offset:12288\n\t"
    "s_waitcnt lgkmcnt(10)\n\t"
    "v_mfma_f32_16x16x32_bf16 v[8:11], v[100:103], v[72:75], v[8:11]\n\t"
    "ds_read_b64_tr_b16 v[124:125], v135 offset:8192\n\t"
    "v_mfma_f32_16x16x32_bf16 v[12:15], v[100:103], v[76:79], v[12:15]\n\t"
    "ds_read_b64_tr_b16 v[126:127], v135 offset:12288\n\t"
    "s_barrier\n\t"
    "s_cmp_lg_u32 %[swave], 0\n\t"
    "s_cbranch_scc1 7f\n\t"
    "s_mov_b64 exec, 1\n\t"
    "buffer_store_dwordx2 v[148:149], v142, %[rf], 0 offen sc1\n\t"
    "s_mov_b64 exec, -1\n\t"
    "7:\n\t"
    "s_waitcnt lgkmcnt(10)\n\t"
    "v_mfma_f32_16x16x32_bf16 v[16:19], v[104:107], v[72:75], v[16:19]\n\t"
    "ds_read_b64_tr_b16 v[96:97], v128 offset:16384\n\t"
    "s_add_u32 m0, %[swv], 57344\n\t"
    "v_mfma_f32_16x16x32_bf16 v[20:23], v[104:107], v[76:79], v[20:23]\n\t"
    "ds_read_b64_tr_b16 v[98:99], v128 offset:20480\n\t"
    "buffer_load_dwordx4 v138, s[40:43], %[sow] offen lds\n\t"
    "s_waitcnt lgkmcnt(10)\n\t"
    "v_mfma_f32_16x16x32_bf16 v[24:27], v[108:111], v[72:75], v[24:27]\n\t"
    "ds_read_b64_tr_b16 v[100:101], v129 offset:16384\n\t"
    "v_mfma_f32_16x16x32_bf16 v[28:31], v[108:111], v[76:79], v[28:31]\n\t"
    "ds_read_b64_tr_b16 v[102:103], v129 offset:20480\n\t"
    "s_waitcnt lgkmcnt(10)\n\t"
    "v_mfma_f32_16x16x32_bf16 v[32:35], v[112:115], v[72:75], v[32:35]\n\t"
    "ds_read_b64_tr_b16 v[104:105], v130 offset:16384\n\t"
    "s_add_u32 m0, %[swv], 61440\n\t"
    "v_mfma_f32_16x16x32_bf16 v[36:39], v[112:115], v[76:79], v[36:39]\n\t"
    "ds_read_b64_tr_b16 v[106:107], v130 offset:20480\n\t"
    "buffer_load_dwordx4 v139, s[40:43], %[sow] offen lds\n\t"
    "s_add_u32 %[sow], %[sow], 131072\n\t"
    "s_waitcnt lgkmcnt(10)\n\t"
    "v_mfma_f32_16x16x32_bf16 v[40:43], v[116:119], v[72:75], v[40:43]\n\t"
    "ds_read_b64_tr_b16 v[108:109], v131 offset:16384\n\t"
    "v_mfma_f32_16x16x32_bf16 v[44:47], v[116:119], v[76:79], v[44:47]\n\t"
    "ds_read_b64_tr_b16 v[110:111], v131 offset:20480\n\t"
    "buffer_load_dwordx4 v[64:67], v136, %[ra], %[soa] offen sc1\n\t"
    "s_waitcnt lgkmcnt(10)\n\t"
    "v_mfma_f32_16x16x32_bf16 v[48:51], v[120:123], v[72:75], v[48:51]\n\t"
    "ds_read_b64_tr_b16 v[112:113], v132 offset:16384\n\t"
    "v_mfma_f32_16x16x32_bf16 v[52:55], v[120:123], v[76:79], v[52:55]\n\t"
    "ds_read_b64_tr_b16 v[114:115], v132 offset:20480\n\t"
    "s_waitcnt lgkmcnt(10)\n\t"
    "v_mfma_f32_16x16x32_bf16 v[56:59], v[124:127], v[72:75], v[56:59]\n\t"
    "ds_read_b64_tr_b16 v[116:117], v133 offset:16384\n\t"
    "v_mfma_f32_16x16x32_bf16 v[60:63], v[124:127], v[76:79], v[60:63]\n\t"
    "ds_read_b64_tr_b16 v[118:119], v133 offset:20480\n\t"
    "buffer_load_dwordx4 v[68:71], v137, %[ra], %[soa] offen sc1\n\t"
    "s_add_u32 %[soa], %[soa], 2048\n\t"
    "s_waitcnt vmcnt(13)\n\t"
    "s_waitcnt lgkmcnt(10)\n\t"
    "v_mfma_f32_16x16x32_bf16 v[0:3], v[96:99], v[80:83], v[0:3]\n\t"
    "ds_read_b64_tr_b16 v[120:121], v134 offset:16384\n\t"
    "s_waitcnt vmcnt(12)\n\t"
    "v_mfma_f32_16x16x32_bf16 v[4:7], v[96:99], v[84:87], v[4:7]\n\t"
    "ds_read_b64_tr_b16 v[122:123], v134 offset:20480\n\t"
    "s_waitcnt lgkmcnt(10)\n\t"
    "v_mfma_f32_16x16x32_bf16 v[8:11], v[100:103], v[80:83], v[8:11]\n\t"
    "ds_read_b64_tr_b16 v[124:125], v135 offset:16384\n\t"
    "v_mfma_f32_16x16x32_bf16 v[12:15], v[100:103], v[84:87], v[12:15]\n\t"
    "ds_read_b64_tr_b16 v[126:127], v135 offset:20480\n\t"
    "s_waitcnt lgkmcnt(10)\n\t"
    "v_mfma_f32_16x16x32_bf16 v[16:19], v[104:107], v[80:83], v[16:19]\n\t"
    "ds_read_b64_tr_b16 v[96:97], v128 offset:24576\n\t"
    "s_add_u32 m0, %[swv], 0\n\t"
    "v_mfma_f32_16x16x32_bf16 v[20:23], v[104:107], v[84:87], v[20:23]\n\t"
    "ds_read_b64_tr_b16 v[98:99], v128 offset:28672\n\t"
    "buffer_load_dwordx4 v138, s[40:43], %[sow] offen lds\n\t"
    "s_waitcnt lgkmcnt(10)\n\t"
    "v_mfma_f32_16x16x32_bf16 v[24:27], v[108:111], v[80:83], v[24:27]\n\t"
    "ds_read_b64_tr_b16 v[100:101], v129 offset:24576\n\t"
    "v_mfma_f32_16x16x32_bf16 v[28:31], v[108:111], v[84:87], v[28:31]\n\t"
    "ds_read_b64_tr_b16 v[102:103], v129 offset:28672\n\t"
    "s_waitcnt lgkmcnt(10)\n\t"
    "v_mfma_f32_16x16x32_bf16 v[32:35], v[112:115], v[80:83], v[32:35]\n\t"
    "ds_read_b64_tr_b16 v[104:105], v130 offset:24576\n\t"
    "s_add_u32 m0, %[swv], 4096\n\t"
    "v_mfma_f32_16x16x32_bf16 v[36:39], v[112:115], v[84:87], v[36:39]\n\t"
    "ds_read_b64_tr_b16 v[106:107], v130 offset:28672\n\t"
    "buffer_load_dwordx4 v139, s[40:43], %[sow] offen lds\n\t"
    "s_add_u32 %[sow], %[sow], 131072\n\t"
    "s_waitcnt lgkmcnt(10)\n\t"
    "v_mfma_f32_16x16x32_bf16 v[40:43], v[116:119], v[80:83], v[40:43]\n\t"
    "ds_read_b64_tr_b16 v[108:109], v131 offset:24576\n\t"
    "v_mfma_f32_16x16x32_bf16 v[44:47], v[116:119], v[84:87], v[44:47]\n\t"
    "ds_read_b64_tr_b16 v[110:111], v131 offset:28672\n\t"
    "buffer_load_dwordx4 v[72:75], v136, %[ra], %[soa] offen sc1\n\t"
    "s_waitcnt lgkmcnt(10)\n\t"
    "v_mfma_f32_16x16x32_bf16 v[48:51], v[120:123], v[80:83], v[48:51]\n\t"
    "ds_read_b64_tr_b16 v[112:113], v132 offset:24576\n\t"
    "v_mfma_f32_16x16x32_bf16 v[52:55], v[120:123], v[84:87], v[52:55]\n\t"
    "ds_read_b64_tr_b16 v[114:115], v132 offset:28672\n\t"
    "s_waitcnt lgkmcnt(10)\n\t"
    "v_mfma_f32_16x16x32_bf16 v[56:59], v[124:127], v[80:83], v[56:59]\n\t"
    "ds_read_b64_tr_b16 v[116:117], v133 offset:24576\n\t"
    "v_mfma_f32_16x16x32_bf16 v[60:63], v[124:127], v[84:87], v[60:63]\n\t"
    "ds_read_b64_tr_b16 v[118:119], v133 offset:28672\n\t"
    "buffer_load_dwordx4 v[76:79], v137, %[ra], %[soa] offen sc1\n\t"
    "s_add_u32 %[soa], %[soa], 2048\n\t"
    "s_waitcnt vmcnt(9)\n\t"
    "s_waitcnt lgkmcnt(10)\n\t"
    "v_mfma_f32_16x16x32_bf16 v[0:3], v[96:99], v[88:91], v[0:3]\n\t"
    "ds_read_b64_tr_b16 v[120:121], v134 offset:24576\n\t"
    "s_waitcnt vmcnt(8)\n\t"
    "v_mfma_f32_16x16x32_bf16 v[4:7], v[96:99], v[92:95], v[4:7]\n\t"
    "ds_read_b64_tr_b16 v[122:123], v134 offset:28672\n\t"
    "s_waitcnt lgkmcnt(10)\n\t"
    "v_mfma_f32_16x16x32_bf16 v[8:11], v[100:103], v[88:91], v[8:11]\n\t"
    "ds_read_b64_tr_b16 v[124:125], v135 offset:24576\n\t"
    "v_mfma_f32_16x16x32_bf16 v[12:15], v[100:103], v[92:95], v[12:15]\n\t"
    "ds_read_b64_tr_b16 v[126:127], v135 offset:28672\n\t"
    "s_barrier\n\t"
    "s_waitcnt lgkmcnt(10)\n\t"
    "v_mfma_f32_16x16x32_bf16 v[16:19], v[104:107], v[88:91], v[16:19]\n\t"
    "ds_read_b64_tr_b16 v[96:97], v128 offset:32768\n\t"
    "s_add_u32 m0, %[swv], 8192\n\t"
    "v_mfma_f32_16x16x32_bf16 v[20:23], v[104:107], v[92:95], v[20:23]\n\t"
    "ds_read_b64_tr_b16 v[98:99], v128 offset:36864\n\t"
    "buffer_load_dwordx4 v138, s[40:43], %[sow] offen lds\n\t"
    "s_waitcnt lgkmcnt(10)\n\t"
    "v_mfma_f32_16x16x32_bf16 v[24:27], v[108:111], v[88:91], v[24:27]\n\t"
    "ds_read_b64_tr_b16 v[100:101], v129 offset:32768\n\t"
    "v_mfma_f32_16x16x32_bf16 v[28:31], v[108:111], v[92:95], v[28:31]\n\t"
    "ds_read_b64_tr_b16 v[102:103], v129 offset:36864\n\t"
    "s_waitcnt lgkmcnt(10)\n\t"
    "v_mfma_f32_16x16x32_bf16 v[32:35], v[112:115], v[88:91], v[32:35]\n\t"
    "ds_read_b64_tr_b16 v[104:105], v130 offset:32768\n\t"
    "s_add_u32 m0, %[swv], 12288\n\t"
    "v_mfma_f32_16x16x32_bf16 v[36:39], v[112:115], v[92:95], v[36:39]\n\t"
    "ds_read_b64_tr_b16 v[106:107], v130 offset:36864\n\t"
    "buffer_load_dwordx4 v139, s[40:43], %[sow] offen lds\n\t"
    "s_add_u32 %[sow], %[sow], 131072\n\t"
    "s_waitcnt lgkmcnt(10)\n\t"
    "v_mfma_f32_16x16x32_bf16 v[40:43], v[116:119], v[88:91], v[40:43]\n\t"
    "ds_read_b64_tr_b16 v[108:109], v131 offset:32768\n\t"
    "v_mfma_f32_16x16x32_bf16 v[44:47], v[116:119], v[92:95], v[44:47]\n\t"
    "ds_read_b64_tr_b16 v[110:111], v131 offset:36864\n\t"
    "buffer_load_dwordx4 v[80:83], v136, %[ra], %[soa] offen sc1\n\t"
    "s_waitcnt lgkmcnt(10)\n\t"
    "v_mfma_f32_16x16x32_bf16 v[48:51], v[120:123], v[88:91], v[48:51]\n\t"
    "ds_read_b64_tr_b16 v[112:113], v132 offset:32768\n\t"
    "v_mfma_f32_16x16x32_bf16 v[52:55], v[120:123], v[92:95], v[52:55]\n\t"
    "ds_read_b64_tr_b16 v[114:115], v132 offset:36864\n\t"
    "s_waitcnt lgkmcnt(10)\n\t"
    "v_mfma_f32_16x16x32_bf16 v[56:59], v[124:127], v[88:91], v[56:59]\n\t"
    "ds_read_b64_tr_b16 v[116:117], v133 offset:32768\n\t"
    "v_mfma_f32_16x16x32_bf16 v[60:63], v[124:127], v[92:95], v[60:63]\n\t"
    "ds_read_b64_tr_b16 v[118:119], v133 offset:36864\n\t"
    "buffer_load_dwordx4 v[84:87], v137, %[ra], %[soa] offen sc1\n\t"
    "s_add_u32 %[soa], %[soa], 2048\n\t"
    "s_waitcnt vmcnt(9)\n\t"
    "s_waitcnt lgkmcnt(10)\n\t"
    "v_mfma_f32_16x16x32_bf16 v[0:3], v[96:99], v[64:67], v[0:3]\n\t"
    "ds_read_b64_tr_b16 v[120:121], v134 offset:32768\n\t"
    "s_waitcnt vmcnt(8)\n\t"
    "v_mfma_f32_16x16x32_bf16 v[4:7], v[96:99], v[68:71], v[4:7]\n\t"
    "ds_read_b64_tr_b16 v[122:123], v134 offset:36864\n\t"
    "s_waitcnt lgkmcnt(10)\n\t"
    "v_mfma_f32_16x16x32_bf16 v[8:11], v[100:103], v[64:67], v[8:11]\n\t"
    "ds_read_b64_tr_b16 v[124:125], v135 offset:32768\n\t"
    "v_mfma_f32_16x16x32_bf16 v[12:15], v[100:103], v[68:71], v[12:15]\n\t"
    "ds_read_b64_tr_b16 v[126:127], v135 offset:36864\n\t"
    "s_waitcnt lgkmcnt(10)\n\t"
    "v_mfma_f32_16x16x32_bf16 v[16:19], v[104:107], v[64:67], v[16:19]\n\t"
    "ds_read_b64_tr_b16 v[96:97], v128 offset:40960\n\t"
    "s_add_u32 m0, %[swv], 16384\n\t"
    "v_mfma_f32_16x16x32_bf16 v[20:23], v[104:107], v[68:71], v[20:23]\n\t"
    "ds_read_b64_tr_b16 v[98:99], v128 offset:45056\n\t"
    "buffer_load_dwordx4 v138, s[40:43], %[sow] offen lds\n\t"
    "s_waitcnt lgkmcnt(10)\n\t"
    "v_mfma_f32_16x16x32_bf16 v[24:27], v[108:111], v[64:67], v[24:27]\n\t"
    "ds_read_b64_tr_b16 v[100:101], v129 offset:40960\n\t"
    "v_mfma_f32_16x16x32_bf16 v[28:31], v[108:111], v[68:71], v[28:31]\n\t"
    "ds_read_b64_tr_b16 v[102:103], v129 offset:45056\n\t"
    "s_waitcnt lgkmcnt(10)\n\t"
    "v_mfma_f32_16x16x32_bf16 v[32:35], v[112:115], v[64:67], v[32:35]\n\t"
    "ds_read_b64_tr_b16 v[104:105], v130 offset:40960\n\t"
    "s_add_u32 m0, %[swv], 20480\n\t"
    "v_mfma_f32_16x16x32_bf16 v[36:39], v[112:115], v[68:71], v[36:39]\n\t"
    "ds_read_b64_tr_b16 v[106:107], v130 offset:45056\n\t"
    "buffer_load_dwordx4 v139, s[40:43], %[sow] offen lds\n\t"
    "s_add_u32 %[sow], %[sow], 131072\n\t"
    "s_waitcnt lgkmcnt(10)\n\t"
    "v_mfma_f32_16x16x32_bf16 v[40:43], v[116:119], v[64:67], v[40:43]\n\t"
    "ds_read_b64_tr_b16 v[108:109], v131 offset:40960\n\t"
    "v_mfma_f32_16x16x32_bf16 v[44:47], v[116:119], v[68:71], v[44:47]\n\t"
    "ds_read_b64_tr_b16 v[110:111], v131 offset:45056\n\t"
    "buffer_load_dwordx4 v[88:91], v136, %[ra], %[soa] offen sc1\n\t"
    "s_waitcnt lgkmcnt(10)\n\t"
    "v_mfma_f32_16x16x32_bf16 v[48:51], v[120:123], v[64:67], v[48:51]\n\t"
    "ds_read_b64_tr_b16 v[112:113], v132 offset:40960\n\t"
    "v_mfma_f32_16x16x32_bf16 v[52:55], v[120:123], v[68:71], v[52:55]\n\t"
    "ds_read_b64_tr_b16 v[114:115], v132 offset:45056\n\t"
    "s_waitcnt lgkmcnt(10)\n\t"
    "v_mfma_f32_16x16x32_bf16 v[56:59], v[124:127], v[64:67], v[56:59]\n\t"
    "ds_read_b64_tr_b16 v[116:117], v133 offset:40960\n\t"
    "v_mfma_f32_16x16x32_bf16 v[60:63], v[124:127], v[68:71], v[60:63]\n\t"
    "ds_read_b64_tr_b16 v[118:119], v133 offset:45056\n\t"
    "buffer_load_dwordx4 v[92:95], v137, %[ra], %[soa] offen sc1\n\t"
    "s_add_u32 %[soa], %[soa], 2048\n\t"
    "s_waitcnt vmcnt(9)\n\t"
    "s_waitcnt lgkmcnt(10)\n\t"
    "v_mfma_f32_16x16x32_bf16 v[0:3], v[96:99], v[72:75], v[0:3]\n\t"
    "ds_read_b64_tr_b16 v[120:121], v134 offset:40960\n\t"
    "s_waitcnt vmcnt(8)\n\t"
    "v_mfma_f32_16x16x32_bf16 v[4:7], v[96:99], v[76:79], v[4:7]\n\t"
    "ds_read_b64_tr_b16 v[122:123], v134 offset:45056\n\t"
    "s_waitcnt lgkmcnt(10)\n\t"
    "v_mfma_f32_16x16x32_bf16 v[8:11], v[100:103], v[72:75], v[8:11]\n\t"
    "ds_read_b64_tr_b16 v[124:125], v135 offset:40960\n\t"
    "v_mfma_f32_16x16x32_bf16 v[12:15], v[100:103], v[76:79], v[12:15]\n\t"
    "ds_read_b64_tr_b16 v[126:127], v135 offset:45056\n\t"
    "s_barrier\n\t"
    "s_waitcnt lgkmcnt(10)\n\t"
    "v_mfma_f32_16x16x32_bf16 v[16:19], v[104:107], v[72:75], v[16:19]\n\t"
    "ds_read_b64_tr_b16 v[96:97], v128 offset:49152\n\t"
    "s_add_u32 m0, %[swv], 24576\n\t"
    "v_mfma_f32_16x16x32_bf16 v[20:23], v[104:107], v[76:79], v[20:23]\n\t"
    "ds_read_b64_tr_b16 v[98:99], v128 offset:53248\n\t"
    "buffer_load_dwordx4 v138, s[40:43], %[sow] offen lds\n\t"
    "s_waitcnt lgkmcnt(10)\n\t"
    "v_mfma_f32_16x16x32_bf16 v[24:27], v[108:111], v[72:75], v[24:27]\n\t"
    "ds_read_b64_tr_b16 v[100:101], v129 offset:49152\n\t"
    "v_mfma_f32_16x16x32_bf16 v[28:31], v[108:111], v[76:79], v[28:31]\n\t"
    "ds_read_b64_tr_b16 v[102:103], v129 offset:53248\n\t"
    "s_waitcnt lgkmcnt(10)\n\t"
    "v_mfma_f32_16x16x32_bf16 v[32:35], v[112:115], v[72:75], v[32:35]\n\t"
    "ds_read_b64_tr_b16 v[104:105], v130 offset:49152\n\t"
    "s_add_u32 m0, %[swv], 28672\n\t"
    "v_mfma_f32_16x16x32_bf16 v[36:39], v[112:115], v[76:79], v[36:39]\n\t"
    "ds_read_b64_tr_b16 v[106:107], v130 offset:53248\n\t"
    "buffer_load_dwordx4 v139, s[40:43], %[sow] offen lds\n\t"
    "s_add_u32 %[sow], %[sow], 131072\n\t"
    "s_waitcnt lgkmcnt(10)\n\t"
    "v_mfma_f32_16x16x32_bf16 v[40:43], v[116:119], v[72:75], v[40:43]\n\t"
    "ds_read_b64_tr_b16 v[108:109], v131 offset:49152\n\t"
    "v_mfma_f32_16x16x32_bf16 v[44:47], v[116:119], v[76:79], v[44:47]\n\t"
    "ds_read_b64_tr_b16 v[110:111], v131 offset:53248\n\t"
    "buffer_load_dwordx4 v[64:67], v136, %[ra], %[soa] offen sc1\n\t"
    "s_waitcnt lgkmcnt(10)\n\t"
    "v_mfma_f32_16x16x32_bf16 v[48:51], v[120:123], v[72:75], v[48:51]\n\t"
    "ds_read_b64_tr_b16 v[112:113], v132 offset:49152\n\t"
    "v_mfma_f32_16x16x32_bf16 v[52:55], v[120:123], v[76:79], v[52:55]\n\t"
    "ds_read_b64_tr_b16 v[114:115], v132 offset:53248\n\t"
    "s_waitcnt lgkmcnt(10)\n\t"
    "v_mfma_f32_16x16x32_bf16 v[56:59], v[124:127], v[72:75], v[56:59]\n\t"
    "ds_read_b64_tr_b16 v[116:117], v133 offset:49152\n\t"
    "v_mfma_f32_16x16x32_bf16 v[60:63], v[124:127], v[76:79], v[60:63]\n\t"
    "ds_read_b64_tr_b16 v[118:119], v133 offset:53248\n\t"
    "buffer_load_dwordx4 v[68:71], v137, %[ra], %[soa] offen sc1\n\t"
    "s_add_u32 %[soa], %[soa], 2048\n\t"
    "s_waitcnt vmcnt(9)\n\t"
    "s_waitcnt lgkmcnt(10)\n\t"
    "v_mfma_f32_16x16x32_bf16 v[0:3], v[96:99], v[80:83], v[0:3]\n\t"
    "ds_read_b64_tr_b16 v[120:121], v134 offset:49152\n\t"
    "s_waitcnt vmcnt(8)\n\t"
    "v_mfma_f32_16x16x32_bf16 v[4:7], v[96:99], v[84:87], v[4:7]\n\t"
    "ds_read_b64_tr_b16 v[122:123], v134 offset:53248\n\t"
    "s_waitcnt lgkmcnt(10)\n\t"
    "v_mfma_f32_16x16x32_bf16 v[8:11], v[100:103], v[80:83], v[8:11]\n\t"
    "ds_read_b64_tr_b16 v[124:125], v135 offset:49152\n\t"
    "v_mfma_f32_16x16x32_bf16 v[12:15], v[100:103], v[84:87], v[12:15]\n\t"
    "ds_read_b64_tr_b16 v[126:127], v135 offset:53248\n\t"
    "s_waitcnt lgkmcnt(10)\n\t"
    "v_mfma_f32_16x16x32_bf16 v[16:19], v[104:107], v[80:83], v[16:19]\n\t"
    "ds_read_b64_tr_b16 v[96:97], v128 offset:57344\n\t"
    "s_add_u32 m0, %[swv], 32768\n\t"
    "v_mfma_f32_16x16x32_bf16 v[20:23], v[104:107], v[84:87], v[20:23]\n\t"
    "ds_read_b64_tr_b16 v[98:99], v128 offset:61440\n\t"
    "buffer_load_dwordx4 v138, s[40:43], %[sow] offen lds\n\t"
    "s_waitcnt lgkmcnt(10)\n\t"
    "v_mfma_f32_16x16x32_bf16 v[24:27], v[108:111], v[80:83], v[24:27]\n\t"
    "ds_read_b64_tr_b16 v[100:101], v129 offset:57344\n\t"
    "v_mfma_f32_16x16x32_bf16 v[28:31], v[108:111], v[84:87], v[28:31]\n\t"
    "ds_read_b64_tr_b16 v[102:103], v129 offset:61440\n\t"
    "s_waitcnt lgkmcnt(10)\n\t"
    "v_mfma_f32_16x16x32_bf16 v[32:35], v[112:115], v[80:83], v[32:35]\n\t"
    "ds_read_b64_tr_b16 v[104:105], v130 offset:57344\n\t"
    "s_add_u32 m0, %[swv], 36864\n\t"
    "v_mfma_f32_16x16x32_bf16 v[36:39], v[112:115], v[84:87], v[36:39]\n\t"
    "ds_read_b64_tr_b16 v[106:107], v130 offset:61440\n\t"
    "buffer_load_dwordx4 v139, s[40:43], %[sow] offen lds\n\t"
    "s_add_u32 %[sow], %[sow], 131072\n\t"
    "s_waitcnt lgkmcnt(10)\n\t"
    "v_mfma_f32_16x16x32_bf16 v[40:43], v[116:119], v[80:83], v[40:43]\n\t"
    "ds_read_b64_tr_b16 v[108:109], v131 offset:57344\n\t"
    "v_mfma_f32_16x16x32_bf16 v[44:47], v[116:119], v[84:87], v[44:47]\n\t"
    "ds_read_b64_tr_b16 v[110:111], v131 offset:61440\n\t"
    "buffer_load_dwordx4 v[72:75], v136, %[ra], %[soa] offen sc1\n\t"
    "s_waitcnt lgkmcnt(10)\n\t"
    "v_mfma_f32_16x16x32_bf16 v[48:51], v[120:123], v[80:83], v[48:51]\n\t"
    "ds_read_b64_tr_b16 v[112:113], v132 offset:57344\n\t"
    "v_mfma_f32_16x16x32_bf16 v[52:55], v[120:123], v[84:87], v[52:55]\n\t"
    "ds_read_b64_tr_b16 v[114:115], v132 offset:61440\n\t"
    "s_waitcnt lgkmcnt(10)\n\t"
    "v_mfma_f32_16x16x32_bf16 v[56:59], v[124:127], v[80:83], v[56:59]\n\t"
    "ds_read_b64_tr_b16 v[116:117], v133 offset:57344\n\t"
    "v_mfma_f32_16x16x32_bf16 v[60:63], v[124:127], v[84:87], v[60:63]\n\t"
    "ds_read_b64_tr_b16 v[118:119], v133 offset:61440\n\t"
    "buffer_load_dwordx4 v[76:79], v137, %[ra], %[soa] offen sc1\n\t"
    "s_add_u32 %[soa], %[soa], 2048\n\t"
    "s_waitcnt vmcnt(9)\n\t"
    "s_waitcnt lgkmcnt(10)\n\t"
    "v_mfma_f32_16x16x32_bf16 v[0:3], v[96:99], v[88:91], v[0:3]\n\t"
    "ds_read_b64_tr_b16 v[120:121], v134 offset:57344\n\t"
    "s_waitcnt vmcnt(8)\n\t"
    "v_mfma_f32_16x16x32_bf16 v[4:7], v[96:99], v[92:95], v[4:7]\n\t"
    "ds_read_b64_tr_b16 v[122:123], v134 offset:61440\n\t"
    "s_waitcnt lgkmcnt(10)\n\t"
    "v_mfma_f32_16x16x32_bf16 v[8:11], v[100:103], v[88:91], v[8:11]\n\t"
    "ds_read_b64_tr_b16 v[124:125], v135 offset:57344\n\t"
    "v_mfma_f32_16x16x32_bf16 v[12:15], v[100:103], v[92:95], v[12:15]\n\t"
    "ds_read_b64_tr_b16 v[126:127], v135 offset:61440\n\t"
    "s_barrier\n\t"
    "s_waitcnt lgkmcnt(10)\n\t"
    "v_mfma_f32_16x16x32_bf16 v[16:19], v[104:107], v[88:91], v[16:19]\n\t"
    "ds_read_b64_tr_b16 v[96:97], v128 offset:0\n\t"
    "s_add_u32 m0, %[swv], 40960\n\t"
    "v_mfma_f32_16x16x32_bf16 v[20:23], v[104:107], v[92:95], v[20:23]\n\t"
    "ds_read_b64_tr_b16 v[98:99], v128 offset:4096\n\t"
    "buffer_load_dwordx4 v138, s[40:43], %[sow] offen lds\n\t"
    "s_waitcnt lgkmcnt(10)\n\t"
    "v_mfma_f32_16x16x32_bf16 v[24:27], v[108:111], v[88:91], v[24:27]\n\t"
    "ds_read_b64_tr_b16 v[100:101], v129 offset:0\n\t"
    "v_mfma_f32_16x16x32_bf16 v[28:31], v[108:111], v[92:95], v[28:31]\n\t"
    "ds_read_b64_tr_b16 v[102:103], v129 offset:4096\n\t"
    "s_waitcnt lgkmcnt(10)\n\t"
    "v_mfma_f32_16x16x32_bf16 v[32:35], v[112:115], v[88:91], v[32:35]\n\t"
    "ds_read_b64_tr_b16 v[104:105], v130 offset:0\n\t"
    "s_add_u32 m0, %[swv], 45056\n\t"
    "v_mfma_f32_16x16x32_bf16 v[36:39], v[112:115], v[92:95], v[36:39]\n\t"
    "ds_read_b64_tr_b16 v[106:107], v130 offset:4096\n\t"
    "buffer_load_dwordx4 v139, s[40:43], %[sow] offen lds\n\t"
    "s_add_u32 %[sow], %[sow], 131072\n\t"
    "s_waitcnt lgkmcnt(10)\n\t"
    "v_mfma_f32_16x16x32_bf16 v[40:43], v[116:119], v[88:91], v[40:43]\n\t"
    "ds_read_b64_tr_b16 v[108:109], v131 offset:0\n\t"
    "v_mfma_f32_16x16x32_bf16 v[44:47], v[116:119], v[92:95], v[44:47]\n\t"
    "ds_read_b64_tr_b16 v[110:111], v131 offset:4096\n\t"
    "buffer_load_dwordx4 v[80:83], v136, %[ra], %[soa] offen sc1\n\t"
    "s_waitcnt lgkmcnt(10)\n\t"
    "v_mfma_f32_16x16x32_bf16 v[48:51], v[120:123], v[88:91], v[48:51]\n\t"
    "ds_read_b64_tr_b16 v[112:113], v132 offset:0\n\t"
    "v_mfma_f32_16x16x32_bf16 v[52:55], v[120:123], v[92:95], v[52:55]\n\t"
    "ds_read_b64_tr_b16 v[114:115], v132 offset:4096\n\t"
    "s_waitcnt lgkmcnt(10)\n\t"
    "v_mfma_f32_16x16x32_bf16 v[56:59], v[124:127], v[88:91], v[56:59]\n\t"
    "ds_read_b64_tr_b16 v[116:117], v133 offset:0\n\t"
    "v_mfma_f32_16x16x32_bf16 v[60:63], v[124:127], v[92:95], v[60:63]\n\t"
    "ds_read_b64_tr_b16 v[118:119], v133 offset:4096\n\t"
    "buffer_load_dwordx4 v[84:87], v137, %[ra], %[soa] offen sc1\n\t"
    "s_add_u32 %[soa], %[soa], 2048\n\t"
    "s_mov_b32 %[cnt], 2\n\t"
    "2:\n\t"
    "s_waitcnt vmcnt(9)\n\t"
    "s_waitcnt lgkmcnt(10)\n\t"
    "v_mfma_f32_16x16x32_bf16 v[0:3], v[96:99], v[64:67], v[0:3]\n\t"
    "ds_read_b64_tr_b16 v[120:121], v134 offset:0\n\t"
    "s_waitcnt vmcnt(8)\n\t"
    "v_mfma_f32_16x16x32_bf16 v[4:7], v[96:99], v[68:71], v[4:7]\n\t"
    "ds_read_b64_tr_b16 v[122:123], v134 offset:4096\n\t"
    "s_waitcnt lgkmcnt(10)\n\t"
    "v_mfma_f32_16x16x32_bf16 v[8:11], v[100:103], v[64:67], v[8:11]\n\t"
    "ds_read_b64_tr_b16 v[124:125], v135 offset:0\n\t"
    "v_mfma_f32_16x16x32_bf16 v[12:15], v[100:103], v[68:71], v[12:15]\n\t"
    "ds_read_b64_tr_b16 v[126:127], v135 offset:4096\n\t"
    "s_waitcnt lgkmcnt(10)\n\t"
    "v_mfma_f32_16x16x32_bf16 v[16:19], v[104:107], v[64:67], v[16:19]\n\t"
    "ds_read_b64_tr_b16 v[96:97], v128 offset:8192\n\t"
    "s_add_u32 m0, %[swv], 49152\n\t"
    "v_mfma_f32_16x16x32_bf16 v[20:23], v[104:107], v[68:71], v[20:23]\n\t"
    "ds_read_b64_tr_b16 v[98:99], v128 offset:12288\n\t"
    "buffer_load_dwordx4 v138, s[40:43], %[sow] offen lds\n\t"
    "s_waitcnt lgkmcnt(10)\n\t"
    "v_mfma_f32_16x16x32_bf16 v[24:27], v[108:111], v[64:67], v[24:27]\n\t"
    "ds_read_b64_tr_b16 v[100:101], v129 offset:8192\n\t"
    "v_mfma_f32_16x16x32_bf16 v[28:31], v[108:111], v[68:71], v[28:31]\n\t"
    "ds_read_b64_tr_b16 v[102:103], v129 offset:12288\n\t"
    "s_waitcnt lgkmcnt(10)\n\t"
    "v_mfma_f32_16x16x32_bf16 v[32:35], v[112:115], v[64:67], v[32:35]\n\t"
    "ds_read_b64_tr_b16 v[104:105], v130 offset:8192\n\t"
    "s_add_u32 m0, %[swv], 53248\n\t"
    "v_mfma_f32_16x16x32_bf16 v[36:39], v[112:115], v[68:71], v[36:39]\n\t"
    "ds_read_b64_tr_b16 v[106:107], v130 offset:12288\n\t"
    "buffer_load_dwordx4 v139, s[40:43], %[sow] offen lds\n\t"
    "s_add_u32 %[sow], %[sow], 131072\n\t"
    "s_waitcnt lgkmcnt(10)\n\t"
    "v_mfma_f32_16x16x32_bf16 v[40:43], v[116:119], v[64:67], v[40:43]\n\t"
    "ds_read_b64_tr_b16 v[108:109], v131 offset:8192\n\t"
    "v_mfma_f32_16x16x32_bf16 v[44:47], v[116:119], v[68:71], v[44:47]\n\t"
    "ds_read_b64_tr_b16 v[110:111], v131 offset:12288\n\t"
    "buffer_load_dwordx4 v[88:91], v136, %[ra], %[soa] offen sc1\n\t"
    "s_waitcnt lgkmcnt(10)\n\t"
    "v_mfma_f32_16x16x32_bf16 v[48:51], v[120:123], v[64:67], v[48:51]\n\t"
    "ds_read_b64_tr_b16 v[112:113], v132 offset:8192\n\t"
    "v_mfma_f32_16x16x32_bf16 v[52:55], v[120:123], v[68:71], v[52:55]\n\t"
    "ds_read_b64_tr_b16 v[114:115], v132 offset:12288\n\t"
    "s_waitcnt lgkmcnt(10)\n\t"
    "v_mfma_f32_16x16x32_bf16 v[56:59], v[124:127], v[64:67], v[56:59]\n\t"
    "ds_read_b64_tr_b16 v[116:117], v133 offset:8192\n\t"
    "v_mfma_f32_16x16x32_bf16 v[60:63], v[124:127], v[68:71], v[60:63]\n\t"
    "ds_read_b64_tr_b16 v[118:119], v133 offset:12288\n\t"
    "buffer_load_dwordx4 v[92:95], v137, %[ra], %[soa] offen sc1\n\t"
    "s_add_u32 %[soa], %[soa], 2048\n\t"
    "s_waitcnt vmcnt(9)\n\t"
    "s_waitcnt lgkmcnt(10)\n\t"
    "v_mfma_f32_16x16x32_bf16 v[0:3], v[96:99], v[72:75], v[0:3]\n\t"
    "ds_read_b64_tr_b16 v[120:121], v134 offset:8192\n\t"
    "s_waitcnt vmcnt(8)\n\t"
    "v_mfma_f32_16x16x32_bf16 v[4:7], v[96:99], v[76:79], v[4:7]\n\t"
    "ds_read_b64_tr_b16 v[122:123], v134 offset:12288\n\t"
    "s_waitcnt lgkmcnt(10)\n\t"
    "v_mfma_f32_16x16x32_bf16 v[8:11], v[100:103], v[72:75], v[8:11]\n\t"
    "ds_read_b64_tr_b16 v[124:125], v135 offset:8192\n\t"
    "v_mfma_f32_16x16x32_bf16 v[12:15], v[100:103], v[76:79], v[12:15]\n\t"
    "ds_read_b64_tr_b16 v[126:127], v135 offset:12288\n\t"
    "s_barrier\n\t"
    "s_waitcnt lgkmcnt(10)\n\t"
    "v_mfma_f32_16x16x32_bf16 v[16:19], v[104:107], v[72:75], v[16:19]\n\t"
    "ds_read_b64_tr_b16 v[96:97], v128 offset:16384\n\t"
    "s_add_u32 m0, %[swv], 57344\n\t"
    "v_mfma_f32_16x16x32_bf16 v[20:23], v[104:107], v[76:79], v[20:23]\n\t"
    "ds_read_b64_tr_b16 v[98:99], v128 offset:20480\n\t"
    "buffer_load_dwordx4 v138, s[40:43], %[sow] offen lds\n\t"
    "s_waitcnt lgkmcnt(10)\n\t"
    "v_mfma_f32_16x16x32_bf16 v[24:27], v[108:111], v[72:75], v[24:27]\n\t"
    "ds_read_b64_tr_b16 v[100:101], v129 offset:16384\n\t"
    "v_mfma_f32_16x16x32_bf16 v[28:31], v[108:111], v[76:79], v[28:31]\n\t"
    "ds_read_b64_tr_b16 v[102:103], v129 offset:20480\n\t"
    "s_waitcnt lgkmcnt(10)\n\t"
    "v_mfma_f32_16x16x32_bf16 v[32:35], v[112:115], v[72:75], v[32:35]\n\t"
    "ds_read_b64_tr_b16 v[104:105], v130 offset:16384\n\t"
    "s_add_u32 m0, %[swv], 61440\n\t"
    "v_mfma_f32_16x16x32_bf16 v[36:39], v[112:115], v[76:79], v[36:39]\n\t"
    "ds_read_b64_tr_b16 v[106:107], v130 offset:20480\n\t"
    "buffer_load_dwordx4 v139, s[40:43], %[sow] offen lds\n\t"
    "s_add_u32 %[sow], %[sow], 131072\n\t"
    "s_waitcnt lgkmcnt(10)\n\t"
    "v_mfma_f32_16x16x32_bf16 v[40:43], v[116:119], v[72:75], v[40:43]\n\t"
    "ds_read_b64_tr_b16 v[108:109], v131 offset:16384\n\t"
    "v_mfma_f32_16x16x32_bf16 v[44:47], v[116:119], v[76:79], v[44:47]\n\t"
    "ds_read_b64_tr_b16 v[110:111], v131 offset:20480\n\t"
    "buffer_load_dwordx4 v[64:67], v136, %[ra], %[soa] offen sc1\n\t"
    "s_waitcnt lgkmcnt(10)\n\t"
    "v_mfma_f32_16x16x32_bf16 v[48:51], v[120:123], v[72:75], v[48:51]\n\t"
    "ds_read_b64_tr_b16 v[112:113], v132 offset:16384\n\t"
    "v_mfma_f32_16x16x32_bf16 v[52:55], v[120:123], v[76:79], v[52:55]\n\t"
    "ds_read_b64_tr_b16 v[114:115], v132 offset:20480\n\t"
    "s_waitcnt lgkmcnt(10)\n\t"
    "v_mfma_f32_16x16x32_bf16 v[56:59], v[124:127], v[72:75], v[56:59]\n\t"
    "ds_read_b64_tr_b16 v[116:117], v133 offset:16384\n\t"
    "v_mfma_f32_16x16x32_bf16 v[60:63], v[124:127], v[76:79], v[60:63]\n\t"
    "ds_read_b64_tr_b16 v[118:119], v133 offset:20480\n\t"
    "buffer_load_dwordx4 v[68:71], v137, %[ra], %[soa] offen sc1\n\t"
    "s_add_u32 %[soa], %[soa], 2048\n\t"
    "s_waitcnt vmcnt(9)\n\t"
    "s_waitcnt lgkmcnt(10)\n\t"
    "v_mfma_f32_16x16x32_bf16 v[0:3], v[96:99], v[80:83], v[0:3]\n\t"
    "ds_read_b64_tr_b16 v[120:121], v134 offset:16384\n\t"
    "s_waitcnt vmcnt(8)\n\t"
    "v_mfma_f32_16x16x32_bf16 v[4:7], v[96:99], v[84:87], v[4:7]\n\t"
    "ds_read_b64_tr_b16 v[122:123], v134 offset:20480\n\t"
    "s_waitcnt lgkmcnt(10)\n\t"
    "v_mfma_f32_16x16x32_bf16 v[8:11], v[100:103], v[80:83], v[8:11]\n\t"
    "ds_read_b64_tr_b16 v[124:125], v135 offset:16384\n\t"
    "v_mfma_f32_16x16x32_bf16 v[12:15], v[100:103], v[84:87], v[12:15]\n\t"
    "ds_read_b64_tr_b16 v[126:127], v135 offset:20480\n\t"
    "s_waitcnt lgkmcnt(10)\n\t"
    "v_mfma_f32_16x16x32_bf16 v[16:19], v[104:107], v[80:83], v[16:19]\n\t"
    "ds_read_b64_tr_b16 v[96:97], v128 offset:24576\n\t"
    "s_add_u32 m0, %[swv], 0\n\t"
    "v_mfma_f32_16x16x32_bf16 v[20:23], v[104:107], v[84:87], v[20:23]\n\t"
    "ds_read_b64_tr_b16 v[98:99], v128 offset:28672\n\t"
    "buffer_load_dwordx4 v138, s[40:43], %[sow] offen lds\n\t"
    "s_waitcnt lgkmcnt(10)\n\t"
    "v_mfma_f32_16x16x32_bf16 v[24:27], v[108:111], v[80:83], v[24:27]\n\t"
    "ds_read_b64_tr_b16 v[100:101], v129 offset:24576\n\t"
    "v_mfma_f32_16x16x32_bf16 v[28:31], v[108:111], v[84:87], v[28:31]\n\t"
    "ds_read_b64_tr_b16 v[102:103], v129 offset:28672\n\t"
    "s_waitcnt lgkmcnt(10)\n\t"
    "v_mfma_f32_16x16x32_bf16 v[32:35], v[112:115], v[80:83], v[32:35]\n\t"
    "ds_read_b64_tr_b16 v[104:105], v130 offset:24576\n\t"
    "s_add_u32 m0, %[swv], 4096\n\t"
    "v_mfma_f32_16x16x32_bf16 v[36:39], v[112:115], v[84:87], v[36:39]\n\t"
    "ds_read_b64_tr_b16 v[106:107], v130 offset:28672\n\t"
    "buffer_load_dwordx4 v139, s[40:43], %[sow] offen lds\n\t"
    "s_add_u32 %[sow], %[sow], 131072\n\t"
    "s_waitcnt lgkmcnt(10)\n\t"
    "v_mfma_f32_16x16x32_bf16 v[40:43], v[116:119], v[80:83], v[40:43]\n\t"
    "ds_read_b64_tr_b16 v[108:109], v131 offset:24576\n\t"
    "v_mfma_f32_16x16x32_bf16 v[44:47], v[116:119], v[84:87], v[44:47]\n\t"
    "ds_read_b64_tr_b16 v[110:111], v131 offset:28672\n\t"
    "buffer_load_dwordx4 v[72:75], v136, %[ra], %[soa] offen sc1\n\t"
    "s_waitcnt lgkmcnt(10)\n\t"
    "v_mfma_f32_16x16x32_bf16 v[48:51], v[120:123], v[80:83], v[48:51]\n\t"
    "ds_read_b64_tr_b16 v[112:113], v132 offset:24576\n\t"
    "v_mfma_f32_16x16x32_bf16 v[52:55], v[120:123], v[84:87], v[52:55]\n\t"
    "ds_read_b64_tr_b16 v[114:115], v132 offset:28672\n\t"
    "s_waitcnt lgkmcnt(10)\n\t"
    "v_mfma_f32_16x16x32_bf16 v[56:59], v[124:127], v[80:83], v[56:59]\n\t"
    "ds_read_b64_tr_b16 v[116:117], v133 offset:24576\n\t"
    "v_mfma_f32_16x16x32_bf16 v[60:63], v[124:127], v[84:87], v[60:63]\n\t"
    "ds_read_b64_tr_b16 v[118:119], v133 offset:28672\n\t"
    "buffer_load_dwordx4 v[76:79], v137, %[ra], %[soa] offen sc1\n\t"
    "s_add_u32 %[soa], %[soa], 2048\n\t"
    "s_waitcnt vmcnt(9)\n\t"
    "s_waitcnt lgkmcnt(10)\n\t"
    "v_mfma_f32_16x16x32_bf16 v[0:3], v[96:99], v[88:91], v[0:3]\n\t"
    "ds_read_b64_tr_b16 v[120:121], v134 offset:24576\n\t"
    "s_waitcnt vmcnt(8)\n\t"
    "v_mfma_f32_16x16x32_bf16 v[4:7], v[96:99], v[92:95], v[4:7]\n\t"
    "ds_read_b64_tr_b16 v[122:123], v134 offset:28672\n\t"
    "s_waitcnt lgkmcnt(10)\n\t"
    "v_mfma_f32_16x16x32_bf16 v[8:11], v[100:103], v[88:91], v[8:11]\n\t"
    "ds_read_b64_tr_b16 v[124:125], v135 offset:24576\n\t"
    "v_mfma_f32_16x16x32_bf16 v[12:15], v[100:103], v[92:95], v[12:15]\n\t"
    "ds_read_b64_tr_b16 v[126:127], v135 offset:28672\n\t"
    "s_barrier\n\t"
    "s_waitcnt lgkmcnt(10)\n\t"
    "v_mfma_f32_16x16x32_bf16 v[16:19], v[104:107], v[88:91], v[16:19]\n\t"
    "ds_read_b64_tr_b16 v[96:97], v128 offset:32768\n\t"
    "s_add_u32 m0, %[swv], 8192\n\t"
    "v_mfma_f32_16x16x32_bf16 v[20:23], v[104:107], v[92:95], v[20:23]\n\t"
    "ds_read_b64_tr_b16 v[98:99], v128 offset:36864\n\t"
    "buffer_load_dwordx4 v138, s[40:43], %[sow] offen lds\n\t"
    "s_waitcnt lgkmcnt(10)\n\t"
    "v_mfma_f32_16x16x32_bf16 v[24:27], v[108:111], v[88:91], v[24:27]\n\t"
    "ds_read_b64_tr_b16 v[100:101], v129 offset:32768\n\t"
    "v_mfma_f32_16x16x32_bf16 v[28:31], v[108:111], v[92:95], v[28:31]\n\t"
    "ds_read_b64_tr_b16 v[102:103], v129 offset:36864\n\t"
    "s_waitcnt lgkmcnt(10)\n\t"
    "v_mfma_f32_16x16x32_bf16 v[32:35], v[112:115], v[88:91], v[32:35]\n\t"
    "ds_read_b64_tr_b16 v[104:105], v130 offset:32768\n\t"
    "s_add_u32 m0, %[swv], 12288\n\t"
    "v_mfma_f32_16x16x32_bf16 v[36:39], v[112:115], v[92:95], v[36:39]\n\t"
    "ds_read_b64_tr_b16 v[106:107], v130 offset:36864\n\t"
    "buffer_load_dwordx4 v139, s[40:43], %[sow] offen lds\n\t"
    "s_add_u32 %[sow], %[sow], 131072\n\t"
    "s_waitcnt lgkmcnt(10)\n\t"
    "v_mfma_f32_16x16x32_bf16 v[40:43], v[116:119], v[88:91], v[40:43]\n\t"
    "ds_read_b64_tr_b16 v[108:109], v131 offset:32768\n\t"
    "v_mfma_f32_16x16x32_bf16 v[44:47], v[116:119], v[92:95], v[44:47]\n\t"
    "ds_read_b64_tr_b16 v[110:111], v131 offset:36864\n\t"
    "buffer_load_dwordx4 v[80:83], v136, %[ra], %[soa] offen sc1\n\t"
    "s_waitcnt lgkmcnt(10)\n\t"
    "v_mfma_f32_16x16x32_bf16 v[48:51], v[120:123], v[88:91], v[48:51]\n\t"
    "ds_read_b64_tr_b16 v[112:113], v132 offset:32768\n\t"
    "v_mfma_f32_16x16x32_bf16 v[52:55], v[120:123], v[92:95], v[52:55]\n\t"
    "ds_read_b64_tr_b16 v[114:115], v132 offset:36864\n\t"
    "s_waitcnt lgkmcnt(10)\n\t"
    "v_mfma_f32_16x16x32_bf16 v[56:59], v[124:127], v[88:91], v[56:59]\n\t"
    "ds_read_b64_tr_b16 v[116:117], v133 offset:32768\n\t"
    "v_mfma_f32_16x16x32_bf16 v[60:63], v[124:127], v[92:95], v[60:63]\n\t"
    "ds_read_b64_tr_b16 v[118:119], v133 offset:36864\n\t"
    "buffer_load_dwordx4 v[84:87], v137, %[ra], %[soa] offen sc1\n\t"
    "s_add_u32 %[soa], %[soa], 2048\n\t"
    "s_waitcnt vmcnt(9)\n\t"
    "s_waitcnt lgkmcnt(10)\n\t"
    "v_mfma_f32_16x16x32_bf16 v[0:3], v[96:99], v[64:67], v[0:3]\n\t"
    "ds_read_b64_tr_b16 v[120:121], v134 offset:32768\n\t"
    "s_waitcnt vmcnt(8)\n\t"
    "v_mfma_f32_16x16x32_bf16 v[4:7], v[96:99], v[68:71], v[4:7]\n\t"
    "ds_read_b64_tr_b16 v[122:123], v134 offset:36864\n\t"
    "s_waitcnt lgkmcnt(10)\n\t"
    "v_mfma_f32_16x16x32_bf16 v[8:11], v[100:103], v[64:67], v[8:11]\n\t"
    "ds_read_b64_tr_b16 v[124:125], v135 offset:32768\n\t"
    "v_mfma_f32_16x16x32_bf16 v[12:15], v[100:103], v[68:71], v[12:15]\n\t"
    "ds_read_b64_tr_b16 v[126:127], v135 offset:36864\n\t"
    "s_waitcnt lgkmcnt(10)\n\t"
    "v_mfma_f32_16x16x32_bf16 v[16:19], v[104:107], v[64:67], v[16:19]\n\t"
    "ds_read_b64_tr_b16 v[96:97], v128 offset:40960\n\t"
    "s_add_u32 m0, %[swv], 16384\n\t"
    "v_mfma_f32_16x16x32_bf16 v[20:23], v[104:107], v[68:71], v[20:23]\n\t"
    "ds_read_b64_tr_b16 v[98:99], v128 offset:45056\n\t"
    "buffer_load_dwordx4 v138, s[40:43], %[sow] offen lds\n\t"
    "s_waitcnt lgkmcnt(10)\n\t"
    "v_mfma_f32_16x16x32_bf16 v[24:27], v[108:111], v[64:67], v[24:27]\n\t"
    "ds_read_b64_tr_b16 v[100:101], v129 offset:40960\n\t"
    "v_mfma_f32_16x16x32_bf16 v[28:31], v[108:111], v[68:71], v[28:31]\n\t"
    "ds_read_b64_tr_b16 v[102:103], v129 offset:45056\n\t"
    "s_waitcnt lgkmcnt(10)\n\t"
    "v_mfma_f32_16x16x32_bf16 v[32:35], v[112:115], v[64:67], v[32:35]\n\t"
    "ds_read_b64_tr_b16 v[104:105], v130 offset:40960\n\t"
    "s_add_u32 m0, %[swv], 20480\n\t"
    "v_mfma_f32_16x16x32_bf16 v[36:39], v[112:115], v[68:71], v[36:39]\n\t"
    "ds_read_b64_tr_b16 v[106:107], v130 offset:45056\n\t"
    "buffer_load_dwordx4 v139, s[40:43], %[sow] offen lds\n\t"
    "s_add_u32 %[sow], %[sow], 131072\n\t"
    "s_waitcnt lgkmcnt(10)\n\t"
    "v_mfma_f32_16x16x32_bf16 v[40:43], v[116:119], v[64:67], v[40:43]\n\t"
    "ds_read_b64_tr_b16 v[108:109], v131 offset:40960\n\t"
    "v_mfma_f32_16x16x32_bf16 v[44:47], v[116:119], v[68:71], v[44:47]\n\t"
    "ds_read_b64_tr_b16 v[110:111], v131 offset:45056\n\t"
    "buffer_load_dwordx4 v[88:91], v136, %[ra], %[soa] offen sc1\n\t"
    "s_waitcnt lgkmcnt(10)\n\t"
    "v_mfma_f32_16x16x32_bf16 v[48:51], v[120:123], v[64:67], v[48:51]\n\t"
    "ds_read_b64_tr_b16 v[112:113], v132 offset:40960\n\t"
    "v_mfma_f32_16x16x32_bf16 v[52:55], v[120:123], v[68:71], v[52:55]\n\t"
    "ds_read_b64_tr_b16 v[114:115], v132 offset:45056\n\t"
    "s_waitcnt lgkmcnt(10)\n\t"
    "v_mfma_f32_16x16x32_bf16 v[56:59], v[124:127], v[64:67], v[56:59]\n\t"
    "ds_read_b64_tr_b16 v[116:117], v133 offset:40960\n\t"
    "v_mfma_f32_16x16x32_bf16 v[60:63], v[124:127], v[68:71], v[60:63]\n\t"
    "ds_read_b64_tr_b16 v[118:119], v133 offset:45056\n\t"
    "buffer_load_dwordx4 v[92:95], v137, %[ra], %[soa] offen sc1\n\t"
    "s_add_u32 %[soa], %[soa], 2048\n\t"
    "s_waitcnt vmcnt(9)\n\t"
    "s_waitcnt lgkmcnt(10)\n\t"
    "v_mfma_f32_16x16x32_bf16 v[0:3], v[96:99], v[72:75], v[0:3]\n\t"
    "ds_read_b64_tr_b16 v[120:121], v134 offset:40960\n\t"
    "s_waitcnt vmcnt(8)\n\t"
    "v_mfma_f32_16x16x32_bf16 v[4:7], v[96:99], v[76:79], v[4:7]\n\t"
    "ds_read_b64_tr_b16 v[122:123], v134 offset:45056\n\t"
    "s_waitcnt lgkmcnt(10)\n\t"
    "v_mfma_f32_16x16x32_bf16 v[8:11], v[100:103], v[72:75], v[8:11]\n\t"
    "ds_read_b64_tr_b16 v[124:125], v135 offset:40960\n\t"
    "v_mfma_f32_16x16x32_bf16 v[12:15], v[100:103], v[76:79], v[12:15]\n\t"
    "ds_read_b64_tr_b16 v[126:127], v135 offset:45056\n\t"
    "s_barrier\n\t"
    "s_waitcnt lgkmcnt(10)\n\t"
    "v_mfma_f32_16x16x32_bf16 v[16:19], v[104:107], v[72:75], v[16:19]\n\t"
    "ds_read_b64_tr_b16 v[96:97], v128 offset:49152\n\t"
    "s_add_u32 m0, %[swv], 24576\n\t"
    "v_mfma_f32_16x16x32_bf16 v[20:23], v[104:107], v[76:79], v[20:23]\n\t"
    "ds_read_b64_tr_b16 v[98:99], v128 offset:53248\n\t"
    "buffer_load_dwordx4 v138, s[40:43], %[sow] offen lds\n\t"
    "s_waitcnt lgkmcnt(10)\n\t"
    "v_mfma_f32_16x16x32_bf16 v[24:27], v[108:111], v[72:75], v[24:27]\n\t"
    "ds_read_b64_tr_b16 v[100:101], v129 offset:49152\n\t"
    "v_mfma_f32_16x16x32_bf16 v[28:31], v[108:111], v[76:79], v[28:31]\n\t"
    "ds_read_b64_tr_b16 v[102:103], v129 offset:53248\n\t"
    "s_waitcnt lgkmcnt(10)\n\t"
    "v_mfma_f32_16x16x32_bf16 v[32:35], v[112:115], v[72:75], v[32:35]\n\t"
    "ds_read_b64_tr_b16 v[104:105], v130 offset:49152\n\t"
    "s_add_u32 m0, %[swv], 28672\n\t"
    "v_mfma_f32_16x16x32_bf16 v[36:39], v[112:115], v[76:79], v[36:39]\n\t"
    "ds_read_b64_tr_b16 v[106:107], v130 offset:53248\n\t"
    "buffer_load_dwordx4 v139, s[40:43], %[sow] offen lds\n\t"
    "s_add_u32 %[sow], %[sow], 131072\n\t"
    "s_waitcnt lgkmcnt(10)\n\t"
    "v_mfma_f32_16x16x32_bf16 v[40:43], v[116:119], v[72:75], v[40:43]\n\t"
    "ds_read_b64_tr_b16 v[108:109], v131 offset:49152\n\t"
    "v_mfma_f32_16x16x32_bf16 v[44:47], v[116:119], v[76:79], v[44:47]\n\t"
    "ds_read_b64_tr_b16 v[110:111], v131 offset:53248\n\t"
    "buffer_load_dwordx4 v[64:67], v136, %[ra], %[soa] offen sc1\n\t"
    "s_waitcnt lgkmcnt(10)\n\t"
    "v_mfma_f32_16x16x32_bf16 v[48:51], v[120:123], v[72:75], v[48:51]\n\t"
    "ds_read_b64_tr_b16 v[112:113], v132 offset:49152\n\t"
    "v_mfma_f32_16x16x32_bf16 v[52:55], v[120:123], v[76:79], v[52:55]\n\t"
    "ds_read_b64_tr_b16 v[114:115], v132 offset:53248\n\t"
    "s_waitcnt lgkmcnt(10)\n\t"
    "v_mfma_f32_16x16x32_bf16 v[56:59], v[124:127], v[72:75], v[56:59]\n\t"
    "ds_read_b64_tr_b16 v[116:117], v133 offset:49152\n\t"
    "v_mfma_f32_16x16x32_bf16 v[60:63], v[124:127], v[76:79], v[60:63]\n\t"
    "ds_read_b64_tr_b16 v[118:119], v133 offset:53248\n\t"
    "buffer_load_dwordx4 v[68:71], v137, %[ra], %[soa] offen sc1\n\t"
    "s_add_u32 %[soa], %[soa], 2048\n\t"
    "s_waitcnt vmcnt(9)\n\t"
    "s_waitcnt lgkmcnt(10)\n\t"
    "v_mfma_f32_16x16x32_bf16 v[0:3], v[96:99], v[80:83], v[0:3]\n\t"
    "ds_read_b64_tr_b16 v[120:121], v134 offset:49152\n\t"
    "s_waitcnt vmcnt(8)\n\t"
    "v_mfma_f32_16x16x32_bf16 v[4:7], v[96:99], v[84:87], v[4:7]\n\t"
    "ds_read_b64_tr_b16 v[122:123], v134 offset:53248\n\t"
    "s_waitcnt lgkmcnt(10)\n\t"
    "v_mfma_f32_16x16x32_bf16 v[8:11], v[100:103], v[80:83], v[8:11]\n\t"
    "ds_read_b64_tr_b16 v[124:125], v135 offset:49152\n\t"
    "v_mfma_f32_16x16x32_bf16 v[12:15], v[100:103], v[84:87], v[12:15]\n\t"
    "ds_read_b64_tr_b16 v[126:127], v135 offset:53248\n\t"
    "s_waitcnt lgkmcnt(10)\n\t"
    "v_mfma_f32_16x16x32_bf16 v[16:19], v[104:107], v[80:83], v[16:19]\n\t"
    "ds_read_b64_tr_b16 v[96:97], v128 offset:57344\n\t"
    "s_cmp_lg_u32 %[cnt], 1\n\t"
    "s_cbranch_scc1 3f\n\t"
    "s_mov_b64 s[40:41], s[44:45]\n\t"
    "s_mov_b64 s[42:43], s[46:47]\n\t"
    "s_mov_b32 %[sow], %[soo]\n\t"
    "3:\n\t"
    "s_add_u32 m0, %[swv], 32768\n\t"
    "v_mfma_f32_16x16x32_bf16 v[20:23], v[104:107], v[84:87], v[20:23]\n\t"
    "ds_read_b64_tr_b16 v[98:99], v128 offset:61440\n\t"
    "buffer_load_dwordx4 v138, s[40:43], %[sow] offen lds\n\t"
    "s_waitcnt lgkmcnt(10)\n\t"
    "v_mfma_f32_16x16x32_bf16 v[24:27], v[108:111], v[80:83], v[24:27]\n\t"
    "ds_read_b64_tr_b16 v[100:101], v129 offset:57344\n\t"
    "v_mfma_f32_16x16x32_bf16 v[28:31], v[108:111], v[84:87], v[28:31]\n\t"
    "ds_read_b64_tr_b16 v[102:103], v129 offset:61440\n\t"
    "s_waitcnt lgkmcnt(10)\n\t"
    "v_mfma_f32_16x16x32_bf16 v[32:35], v[112:115], v[80:83], v[32:35]\n\t"
    "ds_read_b64_tr_b16 v[104:105], v130 offset:57344\n\t"
    "s_add_u32 m0, %[swv], 36864\n\t"
    "v_mfma_f32_16x16x32_bf16 v[36:39], v[112:115], v[84:87], v[36:39]\n\t"
    "ds_read_b64_tr_b16 v[106:107], v130 offset:61440\n\t"
    "buffer_load_dwordx4 v139, s[40:43], %[sow] offen lds\n\t"
    "s_add_u32 %[sow], %[sow], 131072\n\t"
    "s_waitcnt lgkmcnt(10)\n\t"
    "v_mfma_f32_16x16x32_bf16 v[40:43], v[116:119], v[80:83], v[40:43]\n\t"
    "ds_read_b64_tr_b16 v[108:109], v131 offset:57344\n\t"
    "v_mfma_f32_16x16x32_bf16 v[44:47], v[116:119], v[84:87], v[44:47]\n\t"
    "ds_read_b64_tr_b16 v[110:111], v131 offset:61440\n\t"
    "buffer_load_dwordx4 v[72:75], v136, %[ra], %[soa] offen sc1\n\t"
    "s_waitcnt lgkmcnt(10)\n\t"
    "v_mfma_f32_16x16x32_bf16 v[48:51], v[120:123], v[80:83], v[48:51]\n\t"
    "ds_read_b64_tr_b16 v[112:113], v132 offset:57344\n\t"
    "v_mfma_f32_16x16x32_bf16 v[52:55], v[120:123], v[84:87], v[52:55]\n\t"
    "ds_read_b64_tr_b16 v[114:115], v132 offset:61440\n\t"
    "s_waitcnt lgkmcnt(10)\n\t"
    "v_mfma_f32_16x16x32_bf16 v[56:59], v[124:127], v[80:83], v[56:59]\n\t"
    "ds_read_b64_tr_b16 v[116:117], v133 offset:57344\n\t"
    "v_mfma_f32_16x16x32_bf16 v[60:63], v[124:127], v[84:87], v[60:63]\n\t"
    "ds_read_b64_tr_b16 v[118:119], v133 offset:61440\n\t"
    "buffer_load_dwordx4 v[76:79], v137, %[ra], %[soa] offen sc1\n\t"
    "s_add_u32 %[soa], %[soa], 2048\n\t"
    "s_waitcnt vmcnt(9)\n\t"
    "s_waitcnt lgkmcnt(10)\n\t"
    "v_mfma_f32_16x16x32_bf16 v[0:3], v[96:99], v[88:91], v[0:3]\n\t"
    "ds_read_b64_tr_b16 v[120:121], v134 offset:57344\n\t"
    "s_waitcnt vmcnt(8)\n\t"
    "v_mfma_f32_16x16x32_bf16 v[4:7], v[96:99], v[92:95], v[4:7]\n\t"
    "ds_read_b64_tr_b16 v[122:123], v134 offset:61440\n\t"
    "s_waitcnt lgkmcnt(10)\n\t"
    "v_mfma_f32_16x16x32_bf16 v[8:11], v[100:103], v[88:91], v[8:11]\n\t"
    "ds_read_b64_tr_b16 v[124:125], v135 offset:57344\n\t"
    "v_mfma_f32_16x16x32_bf16 v[12:15], v[100:103], v[92:95], v[12:15]\n\t"
    "ds_read_b64_tr_b16 v[126:127], v135 offset:61440\n\t"
    "s_barrier\n\t"
    "s_waitcnt lgkmcnt(10)\n\t"
    "v_mfma_f32_16x16x32_bf16 v[16:19], v[104:107], v[88:91], v[16:19]\n\t"
    "ds_read_b64_tr_b16 v[96:97], v128 offset:0\n\t"
    "s_add_u32 m0, %[swv], 40960\n\t"
    "v_mfma_f32_16x16x32_bf16 v[20:23], v[104:107], v[92:95], v[20:23]\n\t"
    "ds_read_b64_tr_b16 v[98:99], v128 offset:4096\n\t"
    "buffer_load_dwordx4 v138, s[40:43], %[sow] offen lds\n\t"
    "s_waitcnt lgkmcnt(10)\n\t"
    "v_mfma_f32_16x16x32_bf16 v[24:27], v[108:111], v[88:91], v[24:27]\n\t"
    "ds_read_b64_tr_b16 v[100:101], v129 offset:0\n\t"
    "v_mfma_f32_16x16x32_bf16 v[28:31], v[108:111], v[92:95], v[28:31]\n\t"
    "ds_read_b64_tr_b16 v[102:103], v129 offset:4096\n\t"
    "s_waitcnt lgkmcnt(10)\n\t"
    "v_mfma_f32_16x16x32_bf16 v[32:35], v[112:115], v[88:91], v[32:35]\n\t"
    "ds_read_b64_tr_b16 v[104:105], v130 offset:0\n\t"
    "s_add_u32 m0, %[swv], 45056\n\t"
    "v_mfma_f32_16x16x32_bf16 v[36:39], v[112:115], v[92:95], v[36:39]\n\t"
    "ds_read_b64_tr_b16 v[106:107], v130 offset:4096\n\t"
    "buffer_load_dwordx4 v139, s[40:43], %[sow] offen lds\n\t"
    "s_add_u32 %[sow], %[sow], 131072\n\t"
    "s_waitcnt lgkmcnt(10)\n\t"
    "v_mfma_f32_16x16x32_bf16 v[40:43], v[116:119], v[88:91], v[40:43]\n\t"
    "ds_read_b64_tr_b16 v[108:109], v131 offset:0\n\t"
    "v_mfma_f32_16x16x32_bf16 v[44:47], v[116:119], v[92:95], v[44:47]\n\t"
    "ds_read_b64_tr_b16 v[110:111], v131 offset:4096\n\t"
    "buffer_load_dwordx4 v[80:83], v136, %[ra], %[soa] offen sc1\n\t"
    "s_waitcnt lgkmcnt(10)\n\t"
    "v_mfma_f32_16x16x32_bf16 v[48:51], v[120:123], v[88:91], v[48:51]\n\t"
    "ds_read_b64_tr_b16 v[112:113], v132 offset:0\n\t"
    "v_mfma_f32_16x16x32_bf16 v[52:55], v[120:123], v[92:95], v[52:55]\n\t"
    "ds_read_b64_tr_b16 v[114:115], v132 offset:4096\n\t"
    "s_waitcnt lgkmcnt(10)\n\t"
    "v_mfma_f32_16x16x32_bf16 v[56:59], v[124:127], v[88:91], v[56:59]\n\t"
    "ds_read_b64_tr_b16 v[116:117], v133 offset:0\n\t"
    "v_mfma_f32_16x16x32_bf16 v[60:63], v[124:127], v[92:95], v[60:63]\n\t"
    "ds_read_b64_tr_b16 v[118:119], v133 offset:4096\n\t"
    "buffer_load_dwordx4 v[84:87], v137, %[ra], %[soa] offen sc1\n\t"
    "s_add_u32 %[soa], %[soa], 2048\n\t"
    "s_sub_u32 %[cnt], %[cnt], 1\n\t"
    "s_cmp_lg_u32 %[cnt], 0\n\t"
    "s_cbranch_scc1 2b\n\t"
    "s_waitcnt vmcnt(9)\n\t"
    "s_waitcnt lgkmcnt(10)\n\t"
    "v_mfma_f32_16x16x32_bf16 v[0:3], v[96:99], v[64:67], v[0:3]\n\t"
    "ds_read_b64_tr_b16 v[120:121], v134 offset:0\n\t"
    "s_waitcnt vmcnt(8)\n\t"
    "v_mfma_f32_16x16x32_bf16 v[4:7], v[96:99], v[68:71], v[4:7]\n\t"
    "ds_read_b64_tr_b16 v[122:123], v134 offset:4096\n\t"
    "buffer_load_dwordx2 v[146:147], v141, %[rf], 0 offen sc1\n\t"
    "s_waitcnt lgkmcnt(10)\n\t"
    "v_mfma_f32_16x16x32_bf16 v[8:11], v[100:103], v[64:67], v[8:11]\n\t"
    "ds_read_b64_tr_b16 v[124:125], v135 offset:0\n\t"
    "v_mfma_f32_16x16x32_bf16 v[12:15], v[100:103], v[68:71], v[12:15]\n\t"
    "ds_read_b64_tr_b16 v[126:127], v135 offset:4096\n\t"
    "s_waitcnt lgkmcnt(10)\n\t"
    "v_mfma_f32_16x16x32_bf16 v[16:19], v[104:107], v[64:67], v[16:19]\n\t"
    "ds_read_b64_tr_b16 v[96:97], v128 offset:8192\n\t"
    "s_add_u32 m0, %[swv], 49152\n\t"
    "v_mfma_f32_16x16x32_bf16 v[20:23], v[104:107], v[68:71], v[20:23]\n\t"
    "ds_read_b64_tr_b16 v[98:99], v128 offset:12288\n\t"
    "buffer_load_dwordx4 v138, s[40:43], %[sow] offen lds\n\t"
    "s_waitcnt lgkmcnt(10)\n\t"
    "v_mfma_f32_16x16x32_bf16 v[24:27], v[108:111], v[64:67], v[24:27]\n\t"
    "ds_read_b64_tr_b16 v[100:101], v129 offset:8192\n\t"
    "v_mfma_f32_16x16x32_bf16 v[28:31], v[108:111], v[68:71], v[28:31]\n\t"
    "ds_read_b64_tr_b16 v[102:103], v129 offset:12288\n\t"
    "s_waitcnt lgkmcnt(10)\n\t"
    "v_mfma_f32_16x16x32_bf16 v[32:35], v[112:115], v[64:67], v[32:35]\n\t"
    "ds_read_b64_tr_b16 v[104:105], v130 offset:8192\n\t"
    "s_add_u32 m0, %[swv], 53248\n\t"
    "v_mfma_f32_16x16x32_bf16 v[36:39], v[112:115], v[68:71], v[36:39]\n\t"
    "ds_read_b64_tr_b16 v[106:107], v130 offset:12288\n\t"
    "buffer_load_dwordx4 v139, s[40:43], %[sow] offen lds\n\t"
    "s_add_u32 %[sow], %[sow], 131072\n\t"
    "s_waitcnt lgkmcnt(10)\n\t"
    "v_mfma_f32_16x16x32_bf16 v[40:43], v[116:119], v[64:67], v[40:43]\n\t"
    "ds_read_b64_tr_b16 v[108:109], v131 offset:8192\n\t"
    "v_mfma_f32_16x16x32_bf16 v[44:47], v[116:119], v[68:71], v[44:47]\n\t"
    "ds_read_b64_tr_b16 v[110:111], v131 offset:12288\n\t"
    "buffer_load_dwordx4 v[88:91], v136, %[ra], %[soa] offen sc1\n\t"
    "s_waitcnt lgkmcnt(10)\n\t"
    "v_mfma_f32_16x16x32_bf16 v[48:51], v[120:123], v[64:67], v[48:51]\n\t"
    "ds_read_b64_tr_b16 v[112:113], v132 offset:8192\n\t"
    "v_mfma_f32_16x16x32_bf16 v[52:55], v[120:123], v[68:71], v[52:55]\n\t"
    "ds_read_b64_tr_b16 v[114:115], v132 offset:12288\n\t"
    "s_waitcnt lgkmcnt(10)\n\t"
    "v_mfma_f32_16x16x32_bf16 v[56:59], v[124:127], v[64:67], v[56:59]\n\t"
    "ds_read_b64_tr_b16 v[116:117], v133 offset:8192\n\t"
    "v_mfma_f32_16x16x32_bf16 v[60:63], v[124:127], v[68:71], v[60:63]\n\t"
    "ds_read_b64_tr_b16 v[118:119], v133 offset:12288\n\t"
    "buffer_load_dwordx4 v[92:95], v137, %[ra], %[soa] offen sc1\n\t"
    "s_add_u32 %[soa], %[soa], 2048\n\t"
    "s_waitcnt vmcnt(10)\n\t"
    "s_waitcnt lgkmcnt(10)\n\t"
    "v_mfma_f32_16x16x32_bf16 v[0:3], v[96:99], v[72:75], v[0:3]\n\t"
    "ds_read_b64_tr_b16 v[120:121], v134 offset:8192\n\t"
    "s_waitcnt vmcnt(9)\n\t"
    "v_mfma_f32_16x16x32_bf16 v[4:7], v[96:99], v[76:79], v[4:7]\n\t"
    "ds_read_b64_tr_b16 v[122:123], v134 offset:12288\n\t"
    "s_waitcnt lgkmcnt(10)\n\t"
    "v_mfma_f32_16x16x32_bf16 v[8:11], v[100:103], v[72:75], v[8:11]\n\t"
    "ds_read_b64_tr_b16 v[124:125], v135 offset:8192\n\t"
    "v_mfma_f32_16x16x32_bf16 v[12:15], v[100:103], v[76:79], v[12:15]\n\t"
    "ds_read_b64_tr_b16 v[126:127], v135 offset:12288\n\t"
    "s_barrier\n\t"
    "s_waitcnt lgkmcnt(10)\n\t"
    "v_mfma_f32_16x16x32_bf16 v[16:19], v[104:107], v[72:75], v[16:19]\n\t"
    "ds_read_b64_tr_b16 v[96:97], v128 offset:16384\n\t"
    "s_add_u32 m0, %[swv], 57344\n\t"
    "v_mfma_f32_16x16x32_bf16 v[20:23], v[104:107], v[76:79], v[20:23]\n\t"
    "ds_read_b64_tr_b16 v[98:99], v128 offset:20480\n\t"
    "buffer_load_dwordx4 v138, s[40:43], %[sow] offen lds\n\t"
    "s_waitcnt lgkmcnt(10)\n\t"
    "v_mfma_f32_16x16x32_bf16 v[24:27], v[108:111], v[72:75], v[24:27]\n\t"
    "ds_read_b64_tr_b16 v[100:101], v129 offset:16384\n\t"
    "v_mfma_f32_16x16x32_bf16 v[28:31], v[108:111], v[76:79], v[28:31]\n\t"
    "ds_read_b64_tr_b16 v[102:103], v129 offset:20480\n\t"
    "s_waitcnt lgkmcnt(10)\n\t"
    "v_mfma_f32_16x16x32_bf16 v[32:35], v[112:115], v[72:75], v[32:35]\n\t"
    "ds_read_b64_tr_b16 v[104:105], v130 offset:16384\n\t"
    "s_add_u32 m0, %[swv], 61440\n\t"
    "v_mfma_f32_16x16x32_bf16 v[36:39], v[112:115], v[76:79], v[36:39]\n\t"
    "ds_read_b64_tr_b16 v[106:107], v130 offset:20480\n\t"
    "buffer_load_dwordx4 v139, s[40:43], %[sow] offen lds\n\t"
    "s_add_u32 %[sow], %[sow], 131072\n\t"
    "s_waitcnt vmcnt(6)\n\t"
    "v_cmp_ne_u32 vcc, %[tlo], v146\n\t"
    "v_cmp_ne_u32 s[48:49], %[thi], v147\n\t"
    "s_nop 1\n\t"
    "s_or_b64 vcc, vcc, s[48:49]\n\t"
    "s_cbranch_vccz 5f\n\t"
    "s_mov_b32 %[spin], 0x200000\n\t"
    "4:\n\t"
    "s_sleep 2\n\t"
    "buffer_load_dwordx2 v[146:147], v141, %[rf], 0 offen sc1\n\t"
    "s_waitcnt vmcnt(0)\n\t"
    "v_cmp_ne_u32 vcc, %[tlo], v146\n\t"
    "v_cmp_ne_u32 s[48:49], %[thi], v147\n\t"
    "s_nop 1\n\t"
    "s_or_b64 vcc, vcc, s[48:49]\n\t"
    "s_cbranch_vccz 5f\n\t"
    "s_sub_u32 %[spin], %[spin], 1\n\t"
    "s_cmp_lg_u32 %[spin], 0\n\t"
    "s_cbranch_scc1 4b\n\t"
    "5:\n\t"
    "s_waitcnt lgkmcnt(10)\n\t"
    "v_mfma_f32_16x16x32_bf16 v[40:43], v[116:119], v[72:75], v[40:43]\n\t"
    "ds_read_b64_tr_b16 v[108:109], v131 offset:16384\n\t"
    "v_mfma_f32_16x16x32_bf16 v[44:47], v[116:119], v[76:79], v[44:47]\n\t"
    "ds_read_b64_tr_b16 v[110:111], v131 offset:20480\n\t"
    "buffer_load_dwordx4 v[64:67], v136, %[ra], %[soa] offen sc1\n\t"
    "s_waitcnt lgkmcnt(10)\n\t"
    "v_mfma_f32_16x16x32_bf16 v[48:51], v[120:123], v[72:75], v[48:51]\n\t"
    "ds_read_b64_tr_b16 v[112:113], v132 offset:16384\n\t"
    "v_mfma_f32_16x16x32_bf16 v[52:55], v[120:123], v[76:79], v[52:55]\n\t"
    "ds_read_b64_tr_b16 v[114:115], v132 offset:20480\n\t"
    "s_waitcnt lgkmcnt(10)\n\t"
    "v_mfma_f32_16x16x32_bf16 v[56:59], v[124:127], v[72:75], v[56:59]\n\t"
    "ds_read_b64_tr_b16 v[116:117], v133 offset:16384\n\t"
    "v_mfma_f32_16x16x32_bf16 v[60:63], v[124:127], v[76:79], v[60:63]\n\t"
    "ds_read_b64_tr_b16 v[118:119], v133 offset:20480\n\t"
    "buffer_load_dwordx4 v[68:71], v137, %[ra], %[soa] offen sc1\n\t"
    "s_add_u32 %[soa], %[soa], 2048\n\t"
    "s_waitcnt lgkmcnt(10)\n\t"
    "v_mfma_f32_16x16x32_bf16 v[0:3], v[96:99], v[80:83], v[0:3]\n\t"
    "ds_read_b64_tr_b16 v[120:121], v134 offset:16384\n\t"
    "v_mfma_f32_16x16x32_bf16 v[4:7], v[96:99], v[84:87], v[4:7]\n\t"
    "ds_read_b64_tr_b16 v[122:123], v134 offset:20480\n\t"
    "s_waitcnt lgkmcnt(10)\n\t"
    "v_mfma_f32_16x16x32_bf16 v[8:11], v[100:103], v[80:83], v[8:11]\n\t"
    "ds_read_b64_tr_b16 v[124:125], v135 offset:16384\n\t"
    "v_mfma_f32_16x16x32_bf16 v[12:15], v[100:103], v[84:87], v[12:15]\n\t"
    "ds_read_b64_tr_b16 v[126:127], v135 offset:20480\n\t"
    "s_waitcnt lgkmcnt(10)\n\t"
    "v_mfma_f32_16x16x32_bf16 v[16:19], v[104:107], v[80:83], v[16:19]\n\t"
    "ds_read_b64_tr_b16 v[96:97], v128 offset:24576\n\t"
    "s_add_u32 m0, %[swv], 0\n\t"
    "v_mfma_f32_16x16x32_bf16 v[20:23], v[104:107], v[84:87], v[20:23]\n\t"
    "ds_read_b64_tr_b16 v[98:99], v128 offset:28672\n\t"
    "buffer_load_dwordx4 v138, s[40:43], %[sow] offen lds\n\t"
    "s_waitcnt lgkmcnt(10)\n\t"
    "v_mfma_f32_16x16x32_bf16 v[24:27], v[108:111], v[80:83], v[24:27]\n\t"
    "ds_read_b64_tr_b16 v[100:101], v129 offset:24576\n\t"
    "v_mfma_f32_16x16x32_bf16 v[28:31], v[108:111], v[84:87], v[28:31]\n\t"
    "ds_read_b64_tr_b16 v[102:103], v129 offset:28672\n\t"
    "s_waitcnt lgkmcnt(10)\n\t"
    "v_mfma_f32_16x16x32_bf16 v[32:35], v[112:115], v[80:83], v[32:35]\n\t"
    "ds_read_b64_tr_b16 v[104:105], v130 offset:24576\n\t"
    "s_add_u32 m0, %[swv], 4096\n\t"
    "v_mfma_f32_16x16x32_bf16 v[36:39], v[112:115], v[84:87], v[36:39]\n\t"
    "ds_read_b64_tr_b16 v[106:107], v130 offset:28672\n\t"
    "buffer_load_dwordx4 v139, s[40:43], %[sow] offen lds\n\t"
    "s_add_u32 %[sow], %[sow], 131072\n\t"
    "s_waitcnt lgkmcnt(10)\n\t"
    "v_mfma_f32_16x16x32_bf16 v[40:43], v[116:119], v[80:83], v[40:43]\n\t"
    "ds_read_b64_tr_b16 v[108:109], v131 offset:24576\n\t"
    "v_mfma_f32_16x16x32_bf16 v[44:47], v[116:119], v[84:87], v[44:47]\n\t"
    "ds_read_b64_tr_b16 v[110:111], v131 offset:28672\n\t"
    "buffer_load_dwordx4 v[72:75], v136, %[ra], %[soa] offen sc1\n\t"
    "s_waitcnt lgkmcnt(10)\n\t"
    "v_mfma_f32_16x16x32_bf16 v[48:51], v[120:123], v[80:83], v[48:51]\n\t"
    "ds_read_b64_tr_b16 v[112:113], v132 offset:24576\n\t"
    "v_mfma_f32_16x16x32_bf16 v[52:55], v[120:123], v[84:87], v[52:55]\n\t"
    "ds_read_b64_tr_b16 v[114:115], v132 offset:28672\n\t"
    "s_waitcnt lgkmcnt(10)\n\t"
    "v_mfma_f32_16x16x32_bf16 v[56:59], v[124:127], v[80:83], v[56:59]\n\t"
    "ds_read_b64_tr_b16 v[116:117], v133 offset:24576\n\t"
    "v_mfma_f32_16x16x32_bf16 v[60:63], v[124:127], v[84:87], v[60:63]\n\t"
    "ds_read_b64_tr_b16 v[118:119], v133 offset:28672\n\t"
    "buffer_load_dwordx4 v[76:79], v137, %[ra], %[soa] offen sc1\n\t"
    "s_add_u32 %[soa], %[soa], 2048\n\t"
    "s_waitcnt vmcnt(9)\n\t"
    "s_waitcnt lgkmcnt(10)\n\t"
    "v_mfma_f32_16x16x32_bf16 v[0:3], v[96:99], v[88:91], v[0:3]\n\t"
    "ds_read_b64_tr_b16 v[120:121], v134 offset:24576\n\t"
    "s_waitcnt vmcnt(8)\n\t"
    "v_mfma_f32_16x16x32_bf16 v[4:7], v[96:99], v[92:95], v[4:7]\n\t"
    "ds_read_b64_tr_b16 v[122:123], v134 offset:28672\n\t"
    "s_waitcnt lgkmcnt(10)\n\t"
    "v_mfma_f32_16x16x32_bf16 v[8:11], v[100:103], v[88:91], v[8:11]\n\t"
    "ds_read_b64_tr_b16 v[124:125], v135 offset:24576\n\t"
    "v_mfma_f32_16x16x32_bf16 v[12:15], v[100:103], v[92:95], v[12:15]\n\t"
    "ds_read_b64_tr_b16 v[126:127], v135 offset:28672\n\t"
    "s_barrier\n\t"
    "s_waitcnt lgkmcnt(10)\n\t"
    "v_mfma_f32_16x16x32_bf16 v[16:19], v[104:107], v[88:91], v[16:19]\n\t"
    "ds_read_b64_tr_b16 v[96:97], v128 offset:32768\n\t"
    "s_add_u32 m0, %[swv], 8192\n\t"
    "v_mfma_f32_16x16x32_bf16 v[20:23], v[104:107], v[92:95], v[20:23]\n\t"
    "ds_read_b64_tr_b16 v[98:99], v128 offset:36864\n\t"
    "buffer_load_dwordx4 v138, s[40:43], %[sow] offen lds\n\t"
    "s_waitcnt lgkmcnt(10)\n\t"
    "v_mfma_f32_16x16x32_bf16 v[24:27], v[108:111], v[88:91], v[24:27]\n\t"
    "ds_read_b64_tr_b16 v[100:101], v129 offset:32768\n\t"
    "v_mfma_f32_16x16x32_bf16 v[28:31], v[108:111], v[92:95], v[28:31]\n\t"
    "ds_read_b64_tr_b16 v[102:103], v129 offset:36864\n\t"
    "s_waitcnt lgkmcnt(10)\n\t"
    "v_mfma_f32_16x16x32_bf16 v[32:35], v[112:115], v[88:91], v[32:35]\n\t"
    "ds_read_b64_tr_b16 v[104:105], v130 offset:32768\n\t"
    "s_add_u32 m0, %[swv], 12288\n\t"
    "v_mfma_f32_16x16x32_bf16 v[36:39], v[112:115], v[92:95], v[36:39]\n\t"
    "ds_read_b64_tr_b16 v[106:107], v130 offset:36864\n\t"
    "buffer_load_dwordx4 v139, s[40:43], %[sow] offen lds\n\t"
    "s_add_u32 %[sow], %[sow], 131072\n\t"
    "s_waitcnt lgkmcnt(10)\n\t"
    "v_mfma_f32_16x16x32_bf16 v[40:43], v[116:119], v[88:91], v[40:43]\n\t"
    "ds_read_b64_tr_b16 v[108:109], v131 offset:32768\n\t"
    "v_mfma_f32_16x16x32_bf16 v[44:47], v[116:119], v[92:95], v[44:47]\n\t"
    "ds_read_b64_tr_b16 v[110:111], v131 offset:36864\n\t"
    "buffer_load_dwordx4 v[80:83], v136, %[ra], %[soa] offen sc1\n\t"
    "s_waitcnt lgkmcnt(10)\n\t"
    "v_mfma_f32_16x16x32_bf16 v[48:51], v[120:123], v[88:91], v[48:51]\n\t"
    "ds_read_b64_tr_b16 v[112:113], v132 offset:32768\n\t"
    "v_mfma_f32_16x16x32_bf16 v[52:55], v[120:123], v[92:95], v[52:55]\n\t"
    "ds_read_b64_tr_b16 v[114:115], v132 offset:36864\n\t"
    "s_waitcnt lgkmcnt(10)\n\t"
    "v_mfma_f32_16x16x32_bf16 v[56:59], v[124:127], v[88:91], v[56:59]\n\t"
    "ds_read_b64_tr_b16 v[116:117], v133 offset:32768\n\t"
    "v_mfma_f32_16x16x32_bf16 v[60:63], v[124:127], v[92:95], v[60:63]\n\t"
    "ds_read_b64_tr_b16 v[118:119], v133 offset:36864\n\t"
    "buffer_load_dwordx4 v[84:87], v137, %[ra], %[soa] offen sc1\n\t"
    "s_add_u32 %[soa], %[soa], 2048\n\t"
    "s_waitcnt vmcnt(9)\n\t"
    "s_waitcnt lgkmcnt(10)\n\t"
    "v_mfma_f32_16x16x32_bf16 v[0:3], v[96:99], v[64:67], v[0:3]\n\t"
    "ds_read_b64_tr_b16 v[120:121], v134 offset:32768\n\t"
    "s_waitcnt vmcnt(8)\n\t"
    "v_mfma_f32_16x16x32_bf16 v[4:7], v[96:99], v[68:71], v[4:7]\n\t"
    "ds_read_b64_tr_b16 v[122:123], v134 offset:36864\n\t"
    "s_waitcnt lgkmcnt(10)\n\t"
    "v_mfma_f32_16x16x32_bf16 v[8:11], v[100:103], v[64:67], v[8:11]\n\t"
    "ds_read_b64_tr_b16 v[124:125], v135 offset:32768\n\t"
    "v_mfma_f32_16x16x32_bf16 v[12:15], v[100:103], v[68:71], v[12:15]\n\t"
    "ds_read_b64_tr_b16 v[126:127], v135 offset:36864\n\t"
    "s_waitcnt lgkmcnt(10)\n\t"
    "v_mfma_f32_16x16x32_bf16 v[16:19], v[104:107], v[64:67], v[16:19]\n\t"
    "ds_read_b64_tr_b16 v[96:97], v128 offset:40960\n\t"
    "s_add_u32 m0, %[swv], 16384\n\t"
    "v_mfma_f32_16x16x32_bf16 v[20:23], v[104:107], v[68:71], v[20:23]\n\t"
    "ds_read_b64_tr_b16 v[98:99], v128 offset:45056\n\t"
    "buffer_load_dwordx4 v138, s[40:43], %[sow] offen lds\n\t"
    "s_waitcnt lgkmcnt(10)\n\t"
    "v_mfma_f32_16x16x32_bf16 v[24:27], v[108:111], v[64:67], v[24:27]\n\t"
    "ds_read_b64_tr_b16 v[100:101], v129 offset:40960\n\t"
    "v_mfma_f32_16x16x32_bf16 v[28:31], v[108:111], v[68:71], v[28:31]\n\t"
    "ds_read_b64_tr_b16 v[102:103], v129 offset:45056\n\t"
    "s_waitcnt lgkmcnt(10)\n\t"
    "v_mfma_f32_16x16x32_bf16 v[32:35], v[112:115], v[64:67], v[32:35]\n\t"
    "ds_read_b64_tr_b16 v[104:105], v130 offset:40960\n\t"
    "s_add_u32 m0, %[swv], 20480\n\t"
    "v_mfma_f32_16x16x32_bf16 v[36:39], v[112:115], v[68:71], v[36:39]\n\t"
    "ds_read_b64_tr_b16 v[106:107], v130 offset:45056\n\t"
    "buffer_load_dwordx4 v139, s[40:43], %[sow] offen lds\n\t"
    "s_add_u32 %[sow], %[sow], 131072\n\t"
    "s_waitcnt lgkmcnt(10)\n\t"
    "v_mfma_f32_16x16x32_bf16 v[40:43], v[116:119], v[64:67], v[40:43]\n\t"
    "ds_read_b64_tr_b16 v[108:109], v131 offset:40960\n\t"
    "v_mfma_f32_16x16x32_bf16 v[44:47], v[116:119], v[68:71], v[44:47]\n\t"
    "ds_read_b64_tr_b16 v[110:111], v131 offset:45056\n\t"
    "buffer_load_dwordx4 v[88:91], v136, %[ra], %[soa] offen sc1\n\t"
    "s_waitcnt lgkmcnt(10)\n\t"
    "v_mfma_f32_16x16x32_bf16 v[48:51], v[120:123], v[64:67], v[48:51]\n\t"
    "ds_read_b64_tr_b16 v[112:113], v132 offset:40960\n\t"
    "v_mfma_f32_16x16x32_bf16 v[52:55], v[120:123], v[68:71], v[52:55]\n\t"
    "ds_read_b64_tr_b16 v[114:115], v132 offset:45056\n\t"
    "s_waitcnt lgkmcnt(10)\n\t"
    "v_mfma_f32_16x16x32_bf16 v[56:59], v[124:127], v[64:67], v[56:59]\n\t"
    "ds_read_b64_tr_b16 v[116:117], v133 offset:40960\n\t"
    "v_mfma_f32_16x16x32_bf16 v[60:63], v[124:127], v[68:71], v[60:63]\n\t"
    "ds_read_b64_tr_b16 v[118:119], v133 offset:45056\n\t"
    "buffer_load_dwordx4 v[92:95], v137, %[ra], %[soa] offen sc1\n\t"
    "s_add_u32 %[soa], %[soa], 2048\n\t"
    "s_waitcnt vmcnt(9)\n\t"
    "s_waitcnt lgkmcnt(10)\n\t"
    "v_mfma_f32_16x16x32_bf16 v[0:3], v[96:99], v[72:75], v[0:3]\n\t"
    "ds_read_b64_tr_b16 v[120:121], v134 offset:40960\n\t"
    "s_waitcnt vmcnt(8)\n\t"
    "v_mfma_f32_16x16x32_bf16 v[4:7], v[96:99], v[76:79], v[4:7]\n\t"
    "ds_read_b64_tr_b16 v[122:123], v134 offset:45056\n\t"
    "s_waitcnt lgkmcnt(10)\n\t"
    "v_mfma_f32_16x16x32_bf16 v[8:11], v[100:103], v[72:75], v[8:11]\n\t"
    "ds_read_b64_tr_b16 v[124:125], v135 offset:40960\n\t"
    "v_mfma_f32_16x16x32_bf16 v[12:15], v[100:103], v[76:79], v[12:15]\n\t"
    "ds_read_b64_tr_b16 v[126:127], v135 offset:45056\n\t"
    "s_barrier\n\t"
    "s_waitcnt lgkmcnt(10)\n\t"
    "v_mfma_f32_16x16x32_bf16 v[16:19], v[104:107], v[72:75], v[16:19]\n\t"
    "ds_read_b64_tr_b16 v[96:97], v128 offset:49152\n\t"
    "s_add_u32 m0, %[swv], 24576\n\t"
    "v_mfma_f32_16x16x32_bf16 v[20:23], v[104:107], v[76:79], v[20:23]\n\t"
    "ds_read_b64_tr_b16 v[98:99], v128 offset:53248\n\t"
    "buffer_load_dwordx4 v138, s[40:43], %[sow] offen lds\n\t"
    "s_waitcnt lgkmcnt(10)\n\t"
    "v_mfma_f32_16x16x32_bf16 v[24:27], v[108:111], v[72:75], v[24:27]\n\t"
    "ds_read_b64_tr_b16 v[100:101], v129 offset:49152\n\t"
    "v_mfma_f32_16x16x32_bf16 v[28:31], v[108:111], v[76:79], v[28:31]\n\t"
    "ds_read_b64_tr_b16 v[102:103], v129 offset:53248\n\t"
    "s_waitcnt lgkmcnt(10)\n\t"
    "v_mfma_f32_16x16x32_bf16 v[32:35], v[112:115], v[72:75], v[32:35]\n\t"
    "ds_read_b64_tr_b16 v[104:105], v130 offset:49152\n\t"
    "s_add_u32 m0, %[swv], 28672\n\t"
    "v_mfma_f32_16x16x32_bf16 v[36:39], v[112:115], v[76:79], v[36:39]\n\t"
    "ds_read_b64_tr_b16 v[106:107], v130 offset:53248\n\t"
    "buffer_load_dwordx4 v139, s[40:43], %[sow] offen lds\n\t"
    "s_add_u32 %[sow], %[sow], 131072\n\t"
    "s_waitcnt lgkmcnt(10)\n\t"
    "v_mfma_f32_16x16x32_bf16 v[40:43], v[116:119], v[72:75], v[40:43]\n\t"
    "ds_read_b64_tr_b16 v[108:109], v131 offset:49152\n\t"
    "v_mfma_f32_16x16x32_bf16 v[44:47], v[116:119], v[76:79], v[44:47]\n\t"
    "ds_read_b64_tr_b16 v[110:111], v131 offset:53248\n\t"
    "buffer_load_dwordx4 v[64:67], v136, %[ra], %[soa] offen sc1\n\t"
    "s_waitcnt lgkmcnt(10)\n\t"
    "v_mfma_f32_16x16x32_bf16 v[48:51], v[120:123], v[72:75], v[48:51]\n\t"
    "ds_read_b64_tr_b16 v[112:113], v132 offset:49152\n\t"
    "v_mfma_f32_16x16x32_bf16 v[52:55], v[120:123], v[76:79], v[52:55]\n\t"
    "ds_read_b64_tr_b16 v[114:115], v132 offset:53248\n\t"
    "s_waitcnt lgkmcnt(10)\n\t"
    "v_mfma_f32_16x16x32_bf16 v[56:59], v[124:127], v[72:75], v[56:59]\n\t"
    "ds_read_b64_tr_b16 v[116:117], v133 offset:49152\n\t"
    "v_mfma_f32_16x16x32_bf16 v[60:63], v[124:127], v[76:79], v[60:63]\n\t"
    "ds_read_b64_tr_b16 v[118:119], v133 offset:53248\n\t"
    "buffer_load_dwordx4 v[68:71], v137, %[ra], %[soa] offen sc1\n\t"
    "s_add_u32 %[soa], %[soa], 2048\n\t"
    "s_waitcnt vmcnt(9)\n\t"
    "s_waitcnt lgkmcnt(10)\n\t"
    "v_mfma_f32_16x16x32_bf16 v[0:3], v[96:99], v[80:83], v[0:3]\n\t"
    "ds_read_b64_tr_b16 v[120:121], v134 offset:49152\n\t"
    "s_waitcnt vmcnt(8)\n\t"
    "v_mfma_f32_16x16x32_bf16 v[4:7], v[96:99], v[84:87], v[4:7]\n\t"
    "ds_read_b64_tr_b16 v[122:123], v134 offset:53248\n\t"
    "s_waitcnt lgkmcnt(10)\n\t"
    "v_mfma_f32_16x16x32_bf16 v[8:11], v[100:103], v[80:83], v[8:11]\n\t"
    "ds_read_b64_tr_b16 v[124:125], v135 offset:49152\n\t"
    "v_mfma_f32_16x16x32_bf16 v[12:15], v[100:103], v[84:87], v[12:15]\n\t"
    "ds_read_b64_tr_b16 v[126:127], v135 offset:53248\n\t"
    "s_waitcnt lgkmcnt(10)\n\t"
    "v_mfma_f32_16x16x32_bf16 v[16:19], v[104:107], v[80:83], v[16:19]\n\t"
    "ds_read_b64_tr_b16 v[96:97], v128 offset:57344\n\t"
    "v_mfma_f32_16x16x32_bf16 v[20:23], v[104:107], v[84:87], v[20:23]\n\t"
    "ds_read_b64_tr_b16 v[98:99], v128 offset:61440\n\t"
    "s_waitcnt lgkmcnt(10)\n\t"
    "v_mfma_f32_16x16x32_bf16 v[24:27], v[108:111], v[80:83], v[24:27]\n\t"
    "ds_read_b64_tr_b16 v[100:101], v129 offset:57344\n\t"
    "v_mfma_f32_16x16x32_bf16 v[28:31], v[108:111], v[84:87], v[28:31]\n\t"
    "ds_read_b64_tr_b16 v[102:103], v129 offset:61440\n\t"
    "s_waitcnt lgkmcnt(10)\n\t"
    "v_mfma_f32_16x16x32_bf16 v[32:35], v[112:115], v[80:83], v[32:35]\n\t"
    "ds_read_b64_tr_b16 v[104:105], v130 offset:57344\n\t"
    "v_mfma_f32_16x16x32_bf16 v[36:39], v[112:115], v[84:87], v[36:39]\n\t"
    "ds_read_b64_tr_b16 v[106:107], v130 offset:61440\n\t"
    "s_waitcnt lgkmcnt(10)\n\t"
    "v_mfma_f32_16x16x32_bf16 v[40:43], v[116:119], v[80:83], v[40:43]\n\t"
    "ds_read_b64_tr_b16 v[108:109], v131 offset:57344\n\t"
    "v_mfma_f32_16x16x32_bf16 v[44:47], v[116:119], v[84:87], v[44:47]\n\t"
    "ds_read_b64_tr_b16 v[110:111], v131 offset:61440\n\t"
    "buffer_load_dwordx4 v[72:75], v136, %[ra], %[soa] offen sc1\n\t"
    "s_waitcnt lgkmcnt(10)\n\t"
    "v_mfma_f32_16x16x32_bf16 v[48:51], v[120:123], v[80:83], v[48:51]\n\t"
    "ds_read_b64_tr_b16 v[112:113], v132 offset:57344\n\t"
    "v_mfma_f32_16x16x32_bf16 v[52:55], v[120:123], v[84:87], v[52:55]\n\t"
    "ds_read_b64_tr_b16 v[114:115], v132 offset:61440\n\t"
    "s_waitcnt lgkmcnt(10)\n\t"
    "v_mfma_f32_16x16x32_bf16 v[56:59], v[124:127], v[80:83], v[56:59]\n\t"
    "ds_read_b64_tr_b16 v[116:117], v133 offset:57344\n\t"
    "v_mfma_f32_16x16x32_bf16 v[60:63], v[124:127], v[84:87], v[60:63]\n\t"
    "ds_read_b64_tr_b16 v[118:119], v133 offset:61440\n\t"
    "buffer_load_dwordx4 v[76:79], v137, %[ra], %[soa] offen sc1\n\t"
    "s_add_u32 %[soa], %[soa], 2048\n\t"
    "s_waitcnt vmcnt(7)\n\t"
    "s_waitcnt lgkmcnt(10)\n\t"
    "v_mfma_f32_16x16x32_bf16 v[0:3], v[96:99], v[88:91], v[0:3]\n\t"
    "ds_read_b64_tr_b16 v[120:121], v134 offset:57344\n\t"
    "s_waitcnt vmcnt(6)\n\t"
    "v_mfma_f32_16x16x32_bf16 v[4:7], v[96:99], v[92:95], v[4:7]\n\t"
    "ds_read_b64_tr_b16 v[122:123], v134 offset:61440\n\t"
    "s_waitcnt lgkmcnt(10)\n\t"
    "v_mfma_f32_16x16x32_bf16 v[8:11], v[100:103], v[88:91], v[8:11]\n\t"
    "ds_read_b64_tr_b16 v[124:125], v135 offset:57344\n\t"
    "v_mfma_f32_16x16x32_bf16 v[12:15], v[100:103], v[92:95], v[12:15]\n\t"
    "ds_read_b64_tr_b16 v[126:127], v135 offset:61440\n\t"
    "s_barrier\n\t"
    "s_waitcnt lgkmcnt(10)\n\t"
    "v_mfma_f32_16x16x32_bf16 v[16:19], v[104:107], v[88:91], v[16:19]\n\t"
    "ds_read_b64_tr_b16 v[96:97], v128 offset:0\n\t"
    "v_mfma_f32_16x16x32_bf16 v[20:23], v[104:107], v[92:95], v[20:23]\n\t"
    "ds_read_b64_tr_b16 v[98:99], v128 offset:4096\n\t"
    "s_waitcnt lgkmcnt(10)\n\t"
    "v_mfma_f32_16x16x32_bf16 v[24:27], v[108:111], v[88:91], v[24:27]\n\t"
    "ds_read_b64_tr_b16 v[100:101], v129 offset:0\n\t"
    "v_mfma_f32_16x16x32_bf16 v[28:31], v[108:111], v[92:95], v[28:31]\n\t"
    "ds_read_b64_tr_b16 v[102:103], v129 offset:4096\n\t"
    "s_waitcnt lgkmcnt(10)\n\t"
    "v_mfma_f32_16x16x32_bf16 v[32:35], v[112:115], v[88:91], v[32:35]\n\t"
    "ds_read_b64_tr_b16 v[104:105], v130 offset:0\n\t"
    "v_mfma_f32_16x16x32_bf16 v[36:39], v[112:115], v[92:95], v[36:39]\n\t"
    "ds_read_b64_tr_b16 v[106:107], v130 offset:4096\n\t"
    "s_waitcnt lgkmcnt(10)\n\t"
    "v_mfma_f32_16x16x32_bf16 v[40:43], v[116:119], v[88:91], v[40:43]\n\t"
    "ds_read_b64_tr_b16 v[108:109], v131 offset:0\n\t"
    "v_mfma_f32_16x16x32_bf16 v[44:47], v[116:119], v[92:95], v[44:47]\n\t"
    "ds_read_b64_tr_b16 v[110:111], v131 offset:4096\n\t"
    "buffer_load_dwordx4 v[80:83], v136, %[ra], %[soa] offen sc1\n\t"
    "s_waitcnt lgkmcnt(10)\n\t"
    "v_mfma_f32_16x16x32_bf16 v[48:51], v[120:123], v[88:91], v[48:51]\n\t"
    "ds_read_b64_tr_b16 v[112:113], v132 offset:0\n\t"
    "v_mfma_f32_16x16x32_bf16 v[52:55], v[120:123], v[92:95], v[52:55]\n\t"
    "ds_read_b64_tr_b16 v[114:115], v132 offset:4096\n\t"
    "s_waitcnt lgkmcnt(10)\n\t"
    "v_mfma_f32_16x16x32_bf16 v[56:59], v[124:127], v[88:91], v[56:59]\n\t"
    "ds_read_b64_tr_b16 v[116:117], v133 offset:0\n\t"
    "v_mfma_f32_16x16x32_bf16 v[60:63], v[124:127], v[92:95], v[60:63]\n\t"
    "ds_read_b64_tr_b16 v[118:119], v133 offset:4096\n\t"
    "buffer_load_dwordx4 v[84:87], v137, %[ra], %[soa] offen sc1\n\t"
    "s_add_u32 %[soa], %[soa], 2048\n\t"
    "s_waitcnt vmcnt(5)\n\t"
    "s_waitcnt lgkmcnt(10)\n\t"
    "v_mfma_f32_16x16x32_bf16 v[0:3], v[96:99], v[64:67], v[0:3]\n\t"
    "ds_read_b64_tr_b16 v[120:121], v134 offset:0\n\t"
    "s_waitcnt vmcnt(4)\n\t"
    "v_mfma_f32_16x16x32_bf16 v[4:7], v[96:99], v[68:71], v[4:7]\n\t"
    "ds_read_b64_tr_b16 v[122:123], v134 offset:4096\n\t"
    "s_waitcnt lgkmcnt(10)\n\t"
    "v_mfma_f32_16x16x32_bf16 v[8:11], v[100:103], v[64:67], v[8:11]\n\t"
    "ds_read_b64_tr_b16 v[124:125], v135 offset:0\n\t"
    "v_mfma_f32_16x16x32_bf16 v[12:15], v[100:103], v[68:71], v[12:15]\n\t"
    "ds_read_b64_tr_b16 v[126:127], v135 offset:4096\n\t"
    "s_waitcnt lgkmcnt(10)\n\t"
    "v_mfma_f32_16x16x32_bf16 v[16:19], v[104:107], v[64:67], v[16:19]\n\t"
    "ds_read_b64_tr_b16 v[96:97], v128 offset:8192\n\t"
    "v_mfma_f32_16x16x32_bf16 v[20:23], v[104:107], v[68:71], v[20:23]\n\t"
    "ds_read_b64_tr_b16 v[98:99], v128 offset:12288\n\t"
    "s_waitcnt lgkmcnt(10)\n\t"
    "v_mfma_f32_16x16x32_bf16 v[24:27], v[108:111], v[64:67], v[24:27]\n\t"
    "ds_read_b64_tr_b16 v[100:101], v129 offset:8192\n\t"
    "v_mfma_f32_16x16x32_bf16 v[28:31], v[108:111], v[68:71], v[28:31]\n\t"
    "ds_read_b64_tr_b16 v[102:103], v129 offset:12288\n\t"
    "s_waitcnt lgkmcnt(10)\n\t"
    "v_mfma_f32_16x16x32_bf16 v[32:35], v[112:115], v[64:67], v[32:35]\n\t"
    "ds_read_b64_tr_b16 v[104:105], v130 offset:8192\n\t"
    "v_mfma_f32_16x16x32_bf16 v[36:39], v[112:115], v[68:71], v[36:39]\n\t"
    "ds_read_b64_tr_b16 v[106:107], v130 offset:12288\n\t"
    "s_waitcnt lgkmcnt(10)\n\t"
    "v_mfma_f32_16x16x32_bf16 v[40:43], v[116:119], v[64:67], v[40:43]\n\t"
    "ds_read_b64_tr_b16 v[108:109], v131 offset:8192\n\t"
    "v_mfma_f32_16x16x32_bf16 v[44:47], v[116:119], v[68:71], v[44:47]\n\t"
    "ds_read_b64_tr_b16 v[110:111], v131 offset:12288\n\t"
    "buffer_load_dwordx4 v[88:91], v136, %[ra], %[soa] offen sc1\n\t"
    "s_waitcnt lgkmcnt(10)\n\t"
    "v_mfma_f32_16x16x32_bf16 v[48:51], v[120:123], v[64:67], v[48:51]\n\t"
    "ds_read_b64_tr_b16 v[112:113], v132 offset:8192\n\t"
    "v_mfma_f32_16x16x32_bf16 v[52:55], v[120:123], v[68:71], v[52:55]\n\t"
    "ds_read_b64_tr_b16 v[114:115], v132 offset:12288\n\t"
    "s_waitcnt lgkmcnt(10)\n\t"
    "v_mfma_f32_16x16x32_bf16 v[56:59], v[124:127], v[64:67], v[56:59]\n\t"
    "ds_read_b64_tr_b16 v[116:117], v133 offset:8192\n\t"
    "v_mfma_f32_16x16x32_bf16 v[60:63], v[124:127], v[68:71], v[60:63]\n\t"
    "ds_read_b64_tr_b16 v[118:119], v133 offset:12288\n\t"
    "buffer_load_dwordx4 v[92:95], v137, %[ra], %[soa] offen sc1\n\t"
    "s_add_u32 %[soa], %[soa], 2048\n\t"
    "s_waitcnt vmcnt(5)\n\t"
    "s_waitcnt lgkmcnt(10)\n\t"
    "v_mfma_f32_16x16x32_bf16 v[0:3], v[96:99], v[72:75], v[0:3]\n\t"
    "ds_read_b64_tr_b16 v[120:121], v134 offset:8192\n\t"
    "s_waitcnt vmcnt(4)\n\t"
    "v_mfma_f32_16x16x32_bf16 v[4:7], v[96:99], v[76:79], v[4:7]\n\t"
    "ds_read_b64_tr_b16 v[122:123], v134 offset:12288\n\t"
    "s_waitcnt lgkmcnt(10)\n\t"
    "v_mfma_f32_16x16x32_bf16 v[8:11], v[100:103], v[72:75], v[8:11]\n\t"
    "ds_read_b64_tr_b16 v[124:125], v135 offset:8192\n\t"
    "v_mfma_f32_16x16x32_bf16 v[12:15], v[100:103], v[76:79], v[12:15]\n\t"
    "ds_read_b64_tr_b16 v[126:127], v135 offset:12288\n\t"
    "s_barrier\n\t"
    "s_waitcnt lgkmcnt(10)\n\t"
    "v_mfma_f32_16x16x32_bf16 v[16:19], v[104:107], v[72:75], v[16:19]\n\t"
    "ds_read_b64_tr_b16 v[96:97], v128 offset:16384\n\t"
    "v_mfma_f32_16x16x32_bf16 v[20:23], v[104:107], v[76:79], v[20:23]\n\t"
    "ds_read_b64_tr_b16 v[98:99], v128 offset:20480\n\t"
    "s_waitcnt lgkmcnt(10)\n\t"
    "v_mfma_f32_16x16x32_bf16 v[24:27], v[108:111], v[72:75], v[24:27]\n\t"
    "ds_read_b64_tr_b16 v[100:101], v129 offset:16384\n\t"
    "v_mfma_f32_16x16x32_bf16 v[28:31], v[108:111], v[76:79], v[28:31]\n\t"
    "ds_read_b64_tr_b16 v[102:103], v129 offset:20480\n\t"
    "s_waitcnt lgkmcnt(10)\n\t"
    "v_mfma_f32_16x16x32_bf16 v[32:35], v[112:115], v[72:75], v[32:35]\n\t"
    "ds_read_b64_tr_b16 v[104:105], v130 offset:16384\n\t"
    "v_mfma_f32_16x16x32_bf16 v[36:39], v[112:115], v[76:79], v[36:39]\n\t"
    "ds_read_b64_tr_b16 v[106:107], v130 offset:20480\n\t"
    "s_waitcnt lgkmcnt(10)\n\t"
    "v_mfma_f32_16x16x32_bf16 v[40:43], v[116:119], v[72:75], v[40:43]\n\t"
    "ds_read_b64_tr_b16 v[108:109], v131 offset:16384\n\t"
    "v_mfma_f32_16x16x32_bf16 v[44:47], v[116:119], v[76:79], v[44:47]\n\t"
    "ds_read_b64_tr_b16 v[110:111], v131 offset:20480\n\t"
    "s_waitcnt lgkmcnt(10)\n\t"
    "v_mfma_f32_16x16x32_bf16 v[48:51], v[120:123], v[72:75], v[48:51]\n\t"
    "ds_read_b64_tr_b16 v[112:113], v132 offset:16384\n\t"
    "v_mfma_f32_16x16x32_bf16 v[52:55], v[120:123], v[76:79], v[52:55]\n\t"
    "ds_read_b64_tr_b16 v[114:115], v132 offset:20480\n\t"
    "s_waitcnt lgkmcnt(10)\n\t"
    "v_mfma_f32_16x16x32_bf16 v[56:59], v[124:127], v[72:75], v[56:59]\n\t"
    "ds_read_b64_tr_b16 v[116:117], v133 offset:16384\n\t"
    "v_mfma_f32_16x16x32_bf16 v[60:63], v[124:127], v[76:79], v[60:63]\n\t"
    "ds_read_b64_tr_b16 v[118:119], v133 offset:20480\n\t"
    "s_waitcnt vmcnt(3)\n\t"
    "s_waitcnt lgkmcnt(10)\n\t"
    "v_mfma_f32_16x16x32_bf16 v[0:3], v[96:99], v[80:83], v[0:3]\n\t"
    "ds_read_b64_tr_b16 v[120:121], v134 offset:16384\n\t"
    "s_waitcnt vmcnt(2)\n\t"
    "v_mfma_f32_16x16x32_bf16 v[4:7], v[96:99], v[84:87], v[4:7]\n\t"
    "ds_read_b64_tr_b16 v[122:123], v134 offset:20480\n\t"
    "s_waitcnt lgkmcnt(10)\n\t"
    "v_mfma_f32_16x16x32_bf16 v[8:11], v[100:103], v[80:83], v[8:11]\n\t"
    "ds_read_b64_tr_b16 v[124:125], v135 offset:16384\n\t"
    "v_mfma_f32_16x16x32_bf16 v[12:15], v[100:103], v[84:87], v[12:15]\n\t"
    "ds_read_b64_tr_b16 v[126:127], v135 offset:20480\n\t"
    "s_waitcnt lgkmcnt(10)\n\t"
    "v_mfma_f32_16x16x32_bf16 v[16:19], v[104:107], v[80:83], v[16:19]\n\t"
    "ds_read_b64_tr_b16 v[96:97], v128 offset:24576\n\t"
    "v_mfma_f32_16x16x32_bf16 v[20:23], v[104:107], v[84:87], v[20:23]\n\t"
    "ds_read_b64_tr_b16 v[98:99], v128 offset:28672\n\t"
    "s_waitcnt lgkmcnt(10)\n\t"
    "v_mfma_f32_16x16x32_bf16 v[24:27], v[108:111], v[80:83], v[24:27]\n\t"
    "ds_read_b64_tr_b16 v[100:101], v129 offset:24576\n\t"
    "v_mfma_f32_16x16x32_bf16 v[28:31], v[108:111], v[84:87], v[28:31]\n\t"
    "ds_read_b64_tr_b16 v[102:103], v129 offset:28672\n\t"
    "s_waitcnt lgkmcnt(10)\n\t"
    "v_mfma_f32_16x16x32_bf16 v[32:35], v[112:115], v[80:83], v[32:35]\n\t"
    "ds_read_b64_tr_b16 v[104:105], v130 offset:24576\n\t"
    "v_mfma_f32_16x16x32_bf16 v[36:39], v[112:115], v[84:87], v[36:39]\n\t"
    "ds_read_b64_tr_b16 v[106:107], v130 offset:28672\n\t"
    "s_waitcnt lgkmcnt(10)\n\t"
    "v_mfma_f32_16x16x32_bf16 v[40:43], v[116:119], v[80:83], v[40:43]\n\t"
    "ds_read_b64_tr_b16 v[108:109], v131 offset:24576\n\t"
    "v_mfma_f32_16x16x32_bf16 v[44:47], v[116:119], v[84:87], v[44:47]\n\t"
    "ds_read_b64_tr_b16 v[110:111], v131 offset:28672\n\t"
    "s_waitcnt lgkmcnt(10)\n\t"
    "v_mfma_f32_16x16x32_bf16 v[48:51], v[120:123], v[80:83], v[48:51]\n\t"
    "ds_read_b64_tr_b16 v[112:113], v132 offset:24576\n\t"
    "v_mfma_f32_16x16x32_bf16 v[52:55], v[120:123], v[84:87], v[52:55]\n\t"
    "ds_read_b64_tr_b16 v[114:115], v132 offset:28672\n\t"
    "s_waitcnt lgkmcnt(10)\n\t"
    "v_mfma_f32_16x16x32_bf16 v[56:59], v[124:127], v[80:83], v[56:59]\n\t"
    "ds_read_b64_tr_b16 v[116:117], v133 offset:24576\n\t"
    "v_mfma_f32_16x16x32_bf16 v[60:63], v[124:127], v[84:87], v[60:63]\n\t"
    "ds_read_b64_tr_b16 v[118:119], v133 offset:28672\n\t"
    "s_waitcnt vmcnt(1)\n\t"
    "s_waitcnt lgkmcnt(10)\n\t"
    "v_mfma_f32_16x16x32_bf16 v[0:3], v[96:99], v[88:91], v[0:3]\n\t"
    "ds_read_b64_tr_b16 v[120:121], v134 offset:24576\n\t"
    "s_waitcnt vmcnt(0)\n\t"
    "v_mfma_f32_16x16x32_bf16 v[4:7], v[96:99], v[92:95], v[4:7]\n\t"
    "ds_read_b64_tr_b16 v[122:123], v134 offset:28672\n\t"
    "s_waitcnt lgkmcnt(10)\n\t"
    "v_mfma_f32_16x16x32_bf16 v[8:11], v[100:103], v[88:91], v[8:11]\n\t"
    "ds_read_b64_tr_b16 v[124:125], v135 offset:24576\n\t"
    "v_mfma_f32_16x16x32_bf16 v[12:15], v[100:103], v[92:95], v[12:15]\n\t"
    "ds_read_b64_tr_b16 v[126:127], v135 offset:28672\n\t"
    "s_waitcnt lgkmcnt(10)\n\t"
    "v_mfma_f32_16x16x32_bf16 v[16:19], v[104:107], v[88:91], v[16:19]\n\t"
    "v_mfma_f32_16x16x32_bf16 v[20:23], v[104:107], v[92:95], v[20:23]\n\t"
    "s_waitcnt lgkmcnt(8)\n\t"
    "v_mfma_f32_16x16x32_bf16 v[24:27], v[108:111], v[88:91], v[24:27]\n\t"
    "v_mfma_f32_16x16x32_bf16 v[28:31], v[108:111], v[92:95], v[28:31]\n\t"
    "s_waitcnt lgkmcnt(6)\n\t"
    "v_mfma_f32_16x16x32_bf16 v[32:35], v[112:115], v[88:91], v[32:35]\n\t"
    "v_mfma_f32_16x16x32_bf16 v[36:39], v[112:115], v[92:95], v[36:39]\n\t"
    "s_waitcnt lgkmcnt(4)\n\t"
    "v_mfma_f32_16x16x32_bf16 v[40:43], v[116:119], v[88:91], v[40:43]\n\t"
    "v_mfma_f32_16x16x32_bf16 v[44:47], v[116:119], v[92:95], v[44:47]\n\t"
    "s_waitcnt lgkmcnt(2)\n\t"
    "v_mfma_f32_16x16x32_bf16 v[48:51], v[120:123], v[88:91], v[48:51]\n\t"
    "v_mfma_f32_16x16x32_bf16 v[52:55], v[120:123], v[92:95], v[52:55]\n\t"
    "s_waitcnt lgkmcnt(0)\n\t"
    "v_mfma_f32_16x16x32_bf16 v[56:59], v[124:127], v[88:91], v[56:59]\n\t"
    "v_mfma_f32_16x16x32_bf16 v[60:63], v[124:127], v[92:95], v[60:63]\n\t"
    "s_nop 7\n\t"
    "s_nop 7\n\t"
        : "={v[0:15]}"(acc0), "={v[16:31]}"(acc1), "={v[32:47]}"(acc2), "={v[48:63]}"(acc3),
          [soa] "+&s"(soa), [sow] "+&s"(sow), [cnt] "=&s"(cnt), [spin] "=&s"(spin), "+{s[40:43]}"(rw)
        : "{v128}"(vW[0]), "{v129}"(vW[1]), "{v130}"(vW[2]), "{v131}"(vW[3]), "{v132}"(vW[4]), "{v133}"(vW[5]), "{v134}"(vW[6]), "{v135}"(vW[7]), "{v136}"(vAs[0]), "{v137}"(vAs[1]), "{v138}"(vWs[0]), "{v139}"(vWs[1]), "{v141}"(vFo), "{v142}"(vSig), "{v[148:149]}"(vSig2), [ra] "s"(ra), "{s[44:47]}"(ro), [rf] "s"(rf), [swv] "s"(swv), [soo] "s"(soo), [swave] "s"(swave), [tlo] "s"(tlo), [thi] "s"(thi)
        : "memory", "scc", "vcc", "s48", "s49", "v64", "v65", "v66", "v67", "v68", "v69", "v70", "v71", "v72", "v73", "v74", "v75", "v76", "v77", "v78", "v79", "v80", "v81", "v82", "v83", "v84", "v85", "v86", "v87", "v88", "v89", "v90", "v91", "v92", "v93", "v94", "v95", "v96", "v97", "v98", "v99", "v100", "v101", "v102", "v103", "v104", "v105", "v106", "v107", "v108", "v109", "v110", "v111", "v112", "v113", "v114", "v115", "v116", "v117", "v118", "v119", "v120", "v121", "v122", "v123", "v124", "v125", "v126", "v127", "v146", "v147");
    STAMP(3, 4);
    STAMPC(3, 3);
    f32x4 sum[8];
    {
      f32x4* xl = (f32x4*)(smem + (w4 * 2 + (par ^ 1)) * 8192) + lane;
      const f32x4* xr_ = (const f32x4*)(smem + (w4 * 2 + par) * 8192) + lane;
      __syncthreads();
#define K2_T(t, A) { const f32x4 v0 = {A[8 * ((t) & 1)], A[8 * ((t) & 1) + 1], A[8 * ((t) & 1) + 2], A[8 * ((t) & 1) + 3]}; \
                         const f32x4 v1 = {A[8 * ((t) & 1) + 4], A[8 * ((t) & 1) + 5], A[8 * ((t) & 1) + 6], A[8 * ((t) & 1) + 7]}; \
                         sum[t] = par ? v1 : v0; xl[(t) * 64] = par ? v0 : v1; }
      K2_T(0, acc0) K2_T(1, acc0) K2_T(2, acc1) K2_T(3, acc1) K2_T(4, acc2) K2_T(5, acc2) K2_T(6, acc3) K2_T(7, acc3)
#undef K2_T
      __syncthreads();
#pragma unroll
      for (int t = 0; t < 8; ++t) sum[t] += xr_[t * 64];
    }
    f32x4 keep0, keep1, keep2, keep3, s0, s1, s2, s3;
    if (kh) { keep0 = sum[4]; keep1 = sum[5]; keep2 = sum[6]; keep3 = sum[7]; s0 = sum[0]; s1 = sum[1]; s2 = sum[2]; s3 = sum[3]; }
    else { keep0 = sum[0]; keep1 = sum[1]; keep2 = sum[2]; keep3 = sum[3]; s0 = sum[4]; s1 = sum[5]; s2 = sum[6]; s3 = sum[7]; }
    const __amdgpu_buffer_rsrc_t rp = __builtin_amdgcn_make_buffer_rsrc((void*)part, 0, 256u * 32768u, 0x00020000);
    {
      const unsigned so = (b * 8 + w) * (XB16 ? 2048 : 4096) + lane * 16;
      __builtin_amdgcn_raw_buffer_store_b128((u32x4){pk2(s0[0], s0[1]), pk2(s0[2], s0[3]), pk2(s1[0], s1[1]), pk2(s1[2], s1[3])}, rp, so, 0, 16);
      __builtin_amdgcn_raw_buffer_store_b128((u32x4){pk2(s2[0], s2[1]), pk2(s2[2], s2[3]), pk2(s3[0], s3[1]), pk2(s3[2], s3[3])}, rp, so + 1024, 0, 16);
    }
    DRAIN_VM();
    if (lane == 0) st_word(sy, SY_F + b * 128 + F_X + 8 * w, tag);
    wait_tag(sy, SY_F + (b ^ 1) * 128 + F_X + 8 * w, tag);
    {
      const unsigned so = ((b ^ 1) * 8 + w) * (XB16 ? 2048 : 4096) + lane * 16;
      f32x4 pv[4];
      const u32x4 p0 = __builtin_amdgcn_raw_buffer_load_b128(rp, so, 0, 16), p1 = __builtin_amdgcn_raw_buffer_load_b128(rp, so + 1024, 0, 16);
#define UNPK(u) (f32x4){__uint_as_float((u)[0] << 16), __uint_as_float((u)[0] & 0xffff0000u), __uint_as_float((u)[1] << 16), __uint_as_float((u)[1] & 0xffff0000u)}
      pv[0] = UNPK(((u32x2){p0[0], p0[1]})); pv[1] = UNPK(((u32x2){p0[2], p0[3]}));
      pv[2] = UNPK(((u32x2){p1[0], p1[1]})); pv[3] = UNPK(((u32x2){p1[2], p1[3]}));
#undef UNPK
      const __amdgpu_buffer_rsrc_t rout = __builtin_amdgcn_make_buffer_rsrc((void*)out, 0, S_ * D_ * 2, 0x00020000);
#pragma unroll
      for (int tp = 0; tp < 2; ++tp) {
        const f32x4 a = (tp ? keep2 : keep0) + pv[2 * tp], bq = (tp ? keep3 : keep1) + pv[2 * tp + 1];
        float lo[4], hi[4];
#pragma unroll
        for (int i = 0; i < 4; ++i) {
          auto r = __builtin_amdgcn_permlane16_swap(__float_as_uint(a[i]), __float_as_uint(bq[i]), false, false);
          lo[i] = __uint_as_float(r[0]);
          hi[i] = __uint_as_float(r[1]);
        }
        const u32x4 xr = xv[tp];
        const u32x4 o = {pk2(lo[0] + __uint_as_float(xr[0] << 16), lo[1] + __uint_as_float(xr[0] & 0xffff0000u)),
                         pk2(lo[2] + __uint_as_float(xr[1] << 16), lo[3] + __uint_as_float(xr[1] & 0xffff0000u)),
                         pk2(hi[0] + __uint_as_float(xr[2] << 16), hi[1] + __uint_as_float(xr[2] & 0xffff0000u)),
                         pk2(hi[2] + __uint_as_float(xr[3] << 16), hi[3] + __uint_as_float(xr[3] & 0xffff0000u))};
        __builtin_amdgcn_raw_buffer_store_b128(o, rout, xo + tp * 64, 0, OUT_AUX);
      }
    }

    STAMP(3, 1);
  }
  STAMP(1, 1);
  asm volatile(".Lfk_end:");
}

extern "C" void kernel_launch(void* const* d_in, const int* in_sizes, int n_in, void* d_out, int out_size, void* d_ws,
                              size_t ws_size, hipStream_t stream) {
  const u16* x = (const u16*)d_in[0];
  const u16* wqkv = (const u16*)d_in[1];
  const u16* wo = (const u16*)d_in[2];
  const u16* wg = (const u16*)d_in[4];
  const u16* wu = (const u16*)d_in[5];
  const u16* wd = (const u16*)d_in[6];
  fused_kernel<<<256, 512, 0, stream>>>(wqkv, wg, wu, wd, wo, x, (u16*)d_out, (char*)d_ws);
}
```
